```python
import torch

def _softmax(x, axis=-1):
    return torch.softmax(x, dim=axis)

def _gelu(x):
    return torch.nn.functional.gelu(x, approximate='tanh')

def _silu(x):
    return torch.nn.functional.silu(x)

def _sigmoid(x):
    return torch.sigmoid(x)

def _layernorm(x, axis=-1, eps=1e-05):
    m = x.mean(dim=axis, keepdim=True)
    v = x.var(dim=axis, keepdim=True, unbiased=False)
    return (x - m) / torch.sqrt(v + eps)

def _scatter_add(tgt, idx, src, axis=0):
    return tgt.clone().index_add_(axis, idx, src)

def _segment_sum(src, seg, n):
    out = torch.zeros((n,) + src.shape[1:], dtype=src.dtype)
    return out.index_add_(0, seg, src)

def _pad(x, pw):
    flat = tuple((v for pair in reversed(pw) for v in pair))
    return torch.nn.functional.pad(x, flat)
kernel_name = 'adaLN_cond__gqa_block_cached__swiglu_ffn__decode'

def setup_inputs():

    _w = lambda a: (lambda t: t.to(torch.bfloat16) if t.is_floating_point() else t)(torch.as_tensor(a))

    def _A_setup():
        B, Sq, D = (4, 1, 512)
        Hq, Hkv, Sk, Dh = (32, 8, 4096, 128)
        assert Hq % Hkv == 0, (Hq, Hkv)
        return {'x': _w((lambda *s: torch.randn(*s, dtype=torch.bfloat16) * 0.05)(B, Sq, D)), 'k_cache': _w((lambda *s: torch.randn(*s, dtype=torch.bfloat16) * 3.2)(B, Hkv, Sk, Dh)), 'v_cache': _w((lambda *s: torch.randn(*s, dtype=torch.bfloat16) * 0.4)(B, Hkv, Sk, Dh)), 'wq': _w((lambda *s: torch.randn(*s, dtype=torch.bfloat16) * 3.2)(D, Hq * Dh) * D ** (-0.5)), 'wo': _w((lambda *s: torch.randn(*s, dtype=torch.bfloat16) * 0.4)(Hq * Dh, D) * (Hq * Dh) ** (-0.5))}
    _A_in = _A_setup()

    def _B_setup():
        B, S, D, F = (4, 1, 512, 1792)
        return {'x': _w((lambda *s: torch.randn(*s, dtype=torch.bfloat16) * 0.05)(B, S, D)), 'w_gate': _w((lambda *s: torch.randn(*s, dtype=torch.bfloat16) * 0.05)(D, F)), 'w_up': _w((lambda *s: torch.randn(*s, dtype=torch.bfloat16) * 0.05)(D, F)), 'w_down': _w((lambda *s: torch.randn(*s, dtype=torch.bfloat16) * 0.05)(F, D))}
    _B_in = _B_setup()
    return {'A_x': _A_in['x'], 'A_k_cache': _A_in['k_cache'], 'A_v_cache': _A_in['v_cache'], 'A_wq': _A_in['wq'], 'A_wo': _A_in['wo'], 'B_x': _B_in['x'], 'B_w_gate': _B_in['w_gate'], 'B_w_up': _B_in['w_up'], 'B_w_down': _B_in['w_down'], 'c': _w((lambda *s: torch.randn(*s, dtype=torch.bfloat16) * 0.2)(4, 512) * 8.0), 'w_mod': _w((lambda *s: torch.randn(*s, dtype=torch.bfloat16) * 0.2)(512, 6 * 512) * 512 ** (-0.5) * 8.0)}

def reference(A_x, A_k_cache, A_v_cache, A_wq, A_wo, B_x, B_w_gate, B_w_up, B_w_down, c, w_mod):

    def _A_ref(x, k_cache, v_cache, wq, wo):
        B, Sq, D = x.shape
        Hq, Dh = (32, 128)
        Hkv = k_cache.shape[1]
        rep = Hq // Hkv
        q = (lambda x, *d: x.permute(*d))((x @ wq).reshape(B, Sq, Hq, Dh), 0, 2, 1, 3)
        k = (lambda x, n, axis: torch.repeat_interleave(x, n, dim=axis))(k_cache, rep, axis=1)
        v = (lambda x, n, axis: torch.repeat_interleave(x, n, dim=axis))(v_cache, rep, axis=1)
        scores = torch.einsum('bhqd,bhkd->bhqk', q, k) * Dh ** (-0.5)
        p = _softmax(scores, axis=-1)
        o = torch.einsum('bhqk,bhkd->bhqd', p, v)
        return (lambda x, *d: x.permute(*d))(o, 0, 2, 1, 3).reshape(B, Sq, Hq * Dh) @ wo

    def _B_ref(x, w_gate, w_up, w_down):
        g = _silu(x @ w_gate)
        u = x @ w_up
        return g * u @ w_down
    x = A_x
    mod = (_silu(c) @ w_mod).reshape(c.shape[0], 6, 1, -1)
    sa, ca, ga, sb, cb, gb = (mod[:, 0].clone(), mod[:, 1].clone(), mod[:, 2].clone(), mod[:, 3].clone(), mod[:, 4].clone(), mod[:, 5].clone())
    h = _layernorm(x, axis=-1) * (1 + ca) + sa
    x = x + ga * _A_ref(h, A_k_cache, A_v_cache, A_wq, A_wo)
    h = _layernorm(x, axis=-1) * (1 + cb) + sb
    x = x + gb * _B_ref(h, B_w_gate, B_w_up, B_w_down)
    return x
```

```cpp
#include <hip/hip_runtime.h>
#include <hip/hip_bf16.h>
#include <stdint.h>

#define MIMIC 0

typedef unsigned short u16;
__device__ __forceinline__ float bf2f(u16 v) { return __uint_as_float(((unsigned)v) << 16); }
__device__ __forceinline__ u16 f2bf(float f) { __hip_bfloat16 h = __float2bfloat16(f); return *(u16*)&h; }
__device__ __forceinline__ float rb(float f) { return MIMIC ? bf2f(f2bf(f)) : f; }

constexpr int B = 4, D = 512, HQ = 32, HKV = 8, SK = 4096, DH = 128, F = 1792;
constexpr int NS = 8;

__global__ void k_mod(const u16* c, const u16* wmod, float* mod) {
  __shared__ float sc[B][D];
  for (int i = threadIdx.x; i < B * D; i += blockDim.x) { float v = bf2f(c[i]); sc[i / D][i % D] = rb(v / (1.f + __expf(-v))); }
  __syncthreads();
  int n = blockIdx.x * blockDim.x + threadIdx.x;
  float a[B] = {0, 0, 0, 0};
  for (int k = 0; k < D; ++k) { float w = bf2f(wmod[(size_t)k * 6 * D + n]); for (int b = 0; b < B; ++b) a[b] += sc[b][k] * w; }
  for (int b = 0; b < B; ++b) mod[b * 6 * D + n] = rb(a[b]);
}

__device__ void ln_mod(const float* x  , const float* mod, int so, int co, float* h  ) {
  int w = threadIdx.x >> 6, l = threadIdx.x & 63;
  if (w < B) {
    float s = 0; for (int i = l; i < D; i += 64) s += x[w * D + i];
    for (int o = 32; o; o >>= 1) s += __shfl_xor(s, o);
    float mf = s / D; float m = rb(mf);
    float v = 0; for (int i = l; i < D; i += 64) { float d = x[w * D + i] - mf; v += d * d; }
    for (int o = 32; o; o >>= 1) v += __shfl_xor(v, o);
    v = rb(v / D);
    float sd = rb(sqrtf(rb(v + 1e-5f)));
    for (int i = l; i < D; i += 64) {
      float n = rb(rb(x[w * D + i] - m) / sd);
      float t = rb(n * rb(1.f + mod[w * 6 * D + co * D + i]));
      h[w * D + i] = rb(t + mod[w * 6 * D + so * D + i]);
    }
  }
  __syncthreads();
}

__global__ void k_q(const u16* x, const u16* wq, const float* mod, float* q) {
  __shared__ float sx[B * D], sh[B * D];
  for (int i = threadIdx.x; i < B * D; i += blockDim.x) sx[i] = bf2f(x[i]);
  __syncthreads();
  ln_mod(sx, mod, 0, 1, sh);
  int n = blockIdx.x * blockDim.x + threadIdx.x;
  float a[B] = {0, 0, 0, 0};
  for (int k = 0; k < D; ++k) { float w = bf2f(wq[(size_t)k * HQ * DH + n]); for (int b = 0; b < B; ++b) a[b] += sh[b * D + k] * w; }
  for (int b = 0; b < B; ++b) q[b * HQ * DH + n] = rb(a[b]);
}

__global__ void k_attn(const float* q, const u16* K, const u16* V, float* po, float* pml) {
  __shared__ float sq[4][DH];
  __shared__ float ss[4][SK / NS];
  __shared__ float red[4][2];
  int s = blockIdx.x, kvh = blockIdx.y, b = blockIdx.z;
  constexpr int KS = SK / NS;
  for (int i = threadIdx.x; i < 4 * DH; i += 256) sq[i / DH][i % DH] = q[b * HQ * DH + kvh * 4 * DH + i];
  __syncthreads();
  const u16* Kb = K + ((size_t)(b * HKV + kvh) * SK + s * KS) * DH;
  const u16* Vb = V + ((size_t)(b * HKV + kvh) * SK + s * KS) * DH;
  for (int k = threadIdx.x; k < KS; k += 256) {
    float a[4] = {0, 0, 0, 0};
    for (int d = 0; d < DH; ++d) { float kv = bf2f(Kb[(size_t)k * DH + d]); for (int g = 0; g < 4; ++g) a[g] += sq[g][d] * kv; }
    for (int g = 0; g < 4; ++g) ss[g][k] = rb(rb(a[g]) * (MIMIC ? 0.08838834764831845f : 0.08838834764831845f));
  }
  __syncthreads();
  int w = threadIdx.x >> 6, l = threadIdx.x & 63;
  {
    float m = -INFINITY; for (int k = l; k < KS; k += 64) m = fmaxf(m, ss[w][k]);
    for (int o = 32; o; o >>= 1) m = fmaxf(m, __shfl_xor(m, o));
    float sum = 0; for (int k = l; k < KS; k += 64) { float p = __expf(ss[w][k] - m); ss[w][k] = p; sum += p; }
    for (int o = 32; o; o >>= 1) sum += __shfl_xor(sum, o);
    if (l == 0) { red[w][0] = m; red[w][1] = sum; }
  }
  __syncthreads();
  for (int i = threadIdx.x; i < 4 * DH; i += 256) {
    int g = i / DH, d = i % DH; float a = 0;
    for (int k = 0; k < KS; ++k) a += ss[g][k] * bf2f(Vb[(size_t)k * DH + d]);
    po[((size_t)(b * HQ + kvh * 4 + g) * NS + s) * DH + d] = a;
  }
  if (threadIdx.x < 4) { int g = threadIdx.x; pml[((b * HQ + kvh * 4 + g) * NS + s) * 2] = red[g][0]; pml[((b * HQ + kvh * 4 + g) * NS + s) * 2 + 1] = red[g][1]; }
}

__global__ void k_comb(const float* po, const float* pml, float* o) {
  int h = blockIdx.x, b = blockIdx.y, d = threadIdx.x;
  const float* ml = pml + (b * HQ + h) * NS * 2;
  float M = -INFINITY; for (int s = 0; s < NS; ++s) M = fmaxf(M, ml[2 * s]);
  float L = 0, a = 0;
  for (int s = 0; s < NS; ++s) { float wgt = __expf(ml[2 * s] - M); L += wgt * ml[2 * s + 1]; a += wgt * po[((size_t)(b * HQ + h) * NS + s) * DH + d]; }
  o[b * HQ * DH + h * DH + d] = rb(a / L);
}

__global__ void k_wo(const float* o, const u16* wo, float* part) {
  int h = blockIdx.x, n = threadIdx.x;
  float a[B] = {0, 0, 0, 0};
  for (int k = 0; k < DH; ++k) { float w = bf2f(wo[(size_t)(h * DH + k) * D + n]); for (int b = 0; b < B; ++b) a[b] += o[b * HQ * DH + h * DH + k] * w; }
  for (int b = 0; b < B; ++b) part[(h * B + b) * D + n] = a[b];
}

__global__ void k_gu(const u16* x, const float* part, const float* mod, const u16* wg, const u16* wu, float* x1out, float* act) {
  __shared__ float sx[B * D], sh[B * D];
  __shared__ float rg[4][B][64], ru[4][B][64];
  for (int i = threadIdx.x; i < B * D; i += blockDim.x) {
    float a = 0; for (int h = 0; h < HQ; ++h) a += part[h * B * D + i];
    a = rb(a);
    int b = i / D, n = i % D;
    float v = rb(bf2f(x[i]) + rb(mod[b * 6 * D + 2 * D + n] * a));
    sx[i] = v;
    if (blockIdx.x == 0) x1out[i] = v;
  }
  __syncthreads();
  ln_mod(sx, mod, 3, 4, sh);
  int col = threadIdx.x & 63, ks = threadIdx.x >> 6;
  int n = blockIdx.x * 64 + col;
  float g[B] = {0, 0, 0, 0}, u[B] = {0, 0, 0, 0};
  for (int k = ks * 128; k < ks * 128 + 128; ++k) {
    float a = bf2f(wg[(size_t)k * F + n]), c = bf2f(wu[(size_t)k * F + n]);
    for (int b = 0; b < B; ++b) { g[b] += sh[b * D + k] * a; u[b] += sh[b * D + k] * c; }
  }
  for (int b = 0; b < B; ++b) { rg[ks][b][col] = g[b]; ru[ks][b][col] = u[b]; }
  __syncthreads();
  {
    int b = ks;
    float gg = rb(rg[0][b][col] + rg[1][b][col] + rg[2][b][col] + rg[3][b][col]);
    float uu = rb(ru[0][b][col] + ru[1][b][col] + ru[2][b][col] + ru[3][b][col]);
    float sg = rb(gg / (1.f + __expf(-gg)));
    act[b * F + n] = rb(sg * uu);
  }
}

__global__ void k_down(const float* act, const u16* wd, const float* x1, const float* mod, u16* out) {
  __shared__ float r[4][B][64];
  int col = threadIdx.x & 63, ks = threadIdx.x >> 6;
  int n = blockIdx.x * 64 + col;
  float a[B] = {0, 0, 0, 0};
  for (int k = ks * 448; k < ks * 448 + 448; ++k) { float w = bf2f(wd[(size_t)k * D + n]); for (int b = 0; b < B; ++b) a[b] += act[b * F + k] * w; }
  for (int b = 0; b < B; ++b) r[ks][b][col] = a[b];
  __syncthreads();
  int b = ks;
  float y = rb(r[0][b][col] + r[1][b][col] + r[2][b][col] + r[3][b][col]);
  out[b * D + n] = f2bf(x1[b * D + n] + rb(mod[b * 6 * D + 5 * D + n] * y));
}

extern "C" void kernel_launch(void* const* d_in, const int* in_sizes, int n_in,
                              void* d_out, int out_size, void* d_ws, size_t ws_size,
                              hipStream_t stream) {
  const u16* x = (const u16*)d_in[0];
  const u16* K = (const u16*)d_in[1];
  const u16* V = (const u16*)d_in[2];
  const u16* wq = (const u16*)d_in[3];
  const u16* wo = (const u16*)d_in[4];
  const u16* wg = (const u16*)d_in[6];
  const u16* wu = (const u16*)d_in[7];
  const u16* wd = (const u16*)d_in[8];
  const u16* c = (const u16*)d_in[9];
  const u16* wmod = (const u16*)d_in[10];
  float* ws = (float*)d_ws;
  float* mod = ws;
  float* q = mod + 16384;
  float* po = q + 16384;
  float* pml = po + 131072;
  float* o = pml + 4096;
  float* part = o + 16384;
  float* x1 = part + 65536;
  float* act = x1 + 2048;
  k_mod<<<48, 64, 0, stream>>>(c, wmod, mod);
  k_q<<<16, 256, 0, stream>>>(x, wq, mod, q);
  k_attn<<<dim3(NS, HKV, B), 256, 0, stream>>>(q, K, V, po, pml);
  k_comb<<<dim3(HQ, B), 128, 0, stream>>>(po, pml, o);
  k_wo<<<HQ, 512, 0, stream>>>(o, wo, part);
  k_gu<<<28, 256, 0, stream>>>(x, part, mod, wg, wu, x1, act);
  k_down<<<8, 256, 0, stream>>>(act, wd, x1, mod, (u16*)d_out);
}
```

```cpp
#include <hip/hip_runtime.h>
#include <stdint.h>

typedef unsigned int u32;
typedef unsigned long long u64;
typedef u32 v4u __attribute__((ext_vector_type(4)));
typedef u32 v2u __attribute__((ext_vector_type(2)));
typedef float v4f __attribute__((ext_vector_type(4)));
typedef float v2f __attribute__((ext_vector_type(2)));
typedef __bf16 v8bf __attribute__((ext_vector_type(8)));
typedef __bf16 v2bf __attribute__((ext_vector_type(2)));
typedef __amdgpu_buffer_rsrc_t rsrc_t;
#define LAS __attribute__((address_space(3)))
#define CAS __attribute__((address_space(4)))
#define GAS __attribute__((address_space(1)))
#define DEV __device__ __forceinline__
#define CBAR() asm volatile("" ::: "memory")

#define UNITS 0x99999775u
#define WIN 0
#define KSIG 0
#define KBQ 0
#define KBM 4
#define KV_NT 1
#define W_AUX 0
#define PRIO 3
#define DELAY 4
#define QT0 350
#define QR 5
#define QGAP 16
#define HSLEEP 2
#define XSLEEP 4
#define GU_TRIG 0
#define WOT0 1100
#define GUT0 1000
#define ROT 9

constexpr int unit_count(int s) { return (UNITS >> (4 * s)) & 15; }
constexpr int unit_base(int s) { return s == 0 ? 0 : unit_base(s - 1) + unit_count(s - 1); }
constexpr u64 unit_bases() { u64 r = 0; for (int s = 0; s < 8; ++s) r |= (u64)unit_base(s) << (8 * s); return r; }
static_assert(unit_base(8) == 64 && unit_count(0) >= 4 && unit_count(0) <= 5 && unit_count(3) >= 8 && unit_count(4) >= 8, "unit table");

constexpr u32 WS_EPOCH = 0;
constexpr u32 WS_MG = 4096;
constexpr u32 WS_QG = WS_MG + 49152;
constexpr u32 WS_XG = WS_QG + 65536;
constexpr u32 WS_ML = WS_XG + 65536;
constexpr u32 WS_OP = WS_ML + 16384;
constexpr u32 WS_WP = WS_OP + 1048576;
constexpr u32 WS_FP = WS_WP + 524288;
constexpr u32 WS_END = WS_FP + 524288;
constexpr u32 OOB = 0x7ff00000u;

constexpr u32 L_BIG = 5 * 16384;
constexpr u32 L_Q = 147456;
constexpr u32 L_P = L_Q + 1280;
constexpr u32 L_ACT = L_P + 4608;
constexpr u32 L_PART = L_ACT + 4352;
constexpr u32 L_MX = L_PART + 4096;
constexpr u32 L_LS = L_MX + 64;
constexpr u32 L_WS = L_LS + 64;
constexpr u32 L_CTL = L_WS + 128;
constexpr u32 C_TAG = L_CTL, C_GO = L_CTL + 8, C_ISS = L_CTL + 12, C_QRDY = L_CTL + 16, C_K = L_CTL + 20, C_MX = L_CTL + 24,
              C_LS = L_CTL + 28, C_O = L_CTL + 32, C_A = L_CTL + 36, C_P = L_CTL + 40, C_W = L_CTL + 44, C_F = L_CTL + 48,
              C_KI = L_CTL + 52;
constexpr u32 LDS_BYTES = L_CTL + 64;

constexpr u32 SPIN_G = 1u << 18;
constexpr u32 SPIN_L = 1u << 22;

DEV float bflo(u32 v) { return __uint_as_float(v << 16); }
DEV float bfhi(u32 v) { return __uint_as_float(v & 0xffff0000u); }
DEV u32 pk2(float a, float b) { v2f f = {a, b}; v2bf h = __builtin_convertvector(f, v2bf); return __builtin_bit_cast(u32, h); }
DEV float rb(float f) { return bflo(pk2(f, f)); }
DEV v4f mfma(v4u a, v4u b, v4f c) {
  return __builtin_amdgcn_mfma_f32_16x16x32_bf16(__builtin_bit_cast(v8bf, a), __builtin_bit_cast(v8bf, b), c, 0, 0, 0);
}
DEV u32 perm_lo(u32 even, u32 odd) { return __builtin_amdgcn_perm(odd, even, 0x05040100u); }
DEV u32 perm_hi(u32 even, u32 odd) { return __builtin_amdgcn_perm(odd, even, 0x07060302u); }
template <int CTRL> DEV u32 dppu(u32 v) { return __builtin_amdgcn_update_dpp(0u, v, CTRL, 0xf, 0xf, true); }
template <int CTRL> DEV float dppf(float v) { return __uint_as_float(dppu<CTRL>(__float_as_uint(v))); }
DEV float sum16(float v) { u32 x = __float_as_uint(v); auto r = __builtin_amdgcn_permlane16_swap(x, x, false, false); return __uint_as_float(r[0]) + __uint_as_float(r[1]); }
DEV float sum32(float v) { u32 x = __float_as_uint(v); auto r = __builtin_amdgcn_permlane32_swap(x, x, false, false); return __uint_as_float(r[0]) + __uint_as_float(r[1]); }
DEV float max16(float v) { u32 x = __float_as_uint(v); auto r = __builtin_amdgcn_permlane16_swap(x, x, false, false); return fmaxf(__uint_as_float(r[0]), __uint_as_float(r[1])); }
DEV float max32(float v) { u32 x = __float_as_uint(v); auto r = __builtin_amdgcn_permlane32_swap(x, x, false, false); return fmaxf(__uint_as_float(r[0]), __uint_as_float(r[1])); }
DEV float rowsum(float v) {
  v += dppf<0xB1>(v);
  v += dppf<0x4E>(v);
  v += dppf<0x141>(v);
  v += dppf<0x140>(v);
  return v;
}
DEV float silu(float v) { return v * __builtin_amdgcn_rcpf(1.f + __expf(-v)); }

DEV rsrc_t mkrsrc(const void* p, u32 bytes) { return __builtin_amdgcn_make_buffer_rsrc((void*)p, (short)0, (int)bytes, 0x00020000); }

template <typename T> DEV LAS T* lp(LAS char* s, u32 off) { return (LAS T*)(s + off); }

template <int SLEEP> DEV void lds_wait(LAS char* s, u32 off, u32 target) {
#pragma nounroll
  for (u32 i = 0; i < SPIN_L; ++i) {
    u32 v = __builtin_amdgcn_readfirstlane(*(volatile LAS u32*)(s + off));
    if (v >= target) break;
    if (SLEEP) __builtin_amdgcn_s_sleep(SLEEP);
  }
  CBAR();
}
DEV void lds_add(LAS char* s, u32 off, int lane, u32 v) {
  CBAR();
  if (lane == 0) __hip_atomic_fetch_add((LAS u32*)(s + off), v, __ATOMIC_RELAXED, __HIP_MEMORY_SCOPE_WORKGROUP);
  CBAR();
}
DEV u32 lds_inc_ret(LAS char* s, u32 off, int lane) {
  CBAR();
  u32 r = 0;
  if (lane == 0) r = __hip_atomic_fetch_add((LAS u32*)(s + off), 1u, __ATOMIC_RELAXED, __HIP_MEMORY_SCOPE_WORKGROUP);
  r = __builtin_amdgcn_readfirstlane(r);
  CBAR();
  return r;
}
DEV void lds_set(LAS char* s, u32 off) { CBAR(); *(volatile LAS u32*)(s + off) = 1u; CBAR(); }
template <int N, int STRIDE, int SLEEP> DEV void gather2(rsrc_t ws, u32 voff, u32 tag, v4u (&r)[N], u32 vlast) {
#pragma unroll
  for (int i = 0; i < N; ++i) r[i] = __builtin_amdgcn_raw_buffer_load_b128(ws, i == N - 1 ? vlast : voff, i * STRIDE, 16);
#pragma nounroll
  for (u32 it = 0; it < SPIN_G; ++it) {
    bool bad[N], any = false;
#pragma unroll
    for (int i = 0; i < N; ++i) { bad[i] = (r[i].y != tag) | (r[i].w != tag); any = any | bad[i]; }
    if (!__any(any)) break;
    CBAR();
    if (SLEEP) __builtin_amdgcn_s_sleep(SLEEP);
#pragma unroll
    for (int i = 0; i < N; ++i)
      if (bad[i]) r[i] = __builtin_amdgcn_raw_buffer_load_b128(ws, i == N - 1 ? vlast : voff, i * STRIDE, 16);
  }
}
template <int N, int STRIDE, int SLEEP> DEV void gather2(rsrc_t ws, u32 voff, u32 tag, v4u (&r)[N]) { gather2<N, STRIDE, SLEEP>(ws, voff, tag, r, voff); }
template <int OFF> DEV void ldg(v4u& r, rsrc_t ws, u32 vo) {
  asm volatile("buffer_load_dwordx4 %0, %1, %2, 0 offen offset:%3 sc1" : "+v"(r) : "v"(vo), "s"(ws), "i"(OFF) : "memory");
}
template <int N, int STRIDE> DEV void ldset(v4u (&s)[N], rsrc_t ws, const u32 (&vo)[N]) {
  ldg<0>(s[0], ws, vo[0]);
  if constexpr (N > 1) ldg<STRIDE>(s[1], ws, vo[1]);
  if constexpr (N > 2) ldg<2 * STRIDE>(s[2], ws, vo[2]);
  if constexpr (N > 3) ldg<3 * STRIDE>(s[3], ws, vo[3]);
}
template <int N, int STRIDE, int R, int GAP> DEV void gatherp(rsrc_t ws, u32 voff, u32 vlast, u32 tag, v2u (&res)[N], v4u (&s)[R][N]) {
  u32 vo[N];
#pragma unroll
  for (int i = 0; i < N; ++i) { vo[i] = i == N - 1 ? vlast : voff; res[i] = v2u{0u, 0u}; }
#pragma unroll
  for (int r = 0; r < R; ++r) {
#pragma unroll
    for (int i = 0; i < N; ++i) asm volatile("" : "=v"(s[r][i]));
    ldset<N, STRIDE>(s[r], ws, vo);
    if (r < R - 1) __builtin_amdgcn_s_sleep(GAP);
  }
#pragma nounroll
  for (u32 it = 0; it < SPIN_G; ++it) {
#pragma unroll
    for (int r = 0; r < R; ++r) {
      asm volatile("s_waitcnt vmcnt(%0)" :: "i"((R - 1) * N) : "memory");
      bool any = false;
#pragma unroll
      for (int i = 0; i < N; ++i) {
        asm volatile("" : "+v"(s[r][i]));
        const bool good = (vo[i] != OOB) & (s[r][i].y == tag) & (s[r][i].w == tag);
        res[i].x = good ? s[r][i].x : res[i].x; res[i].y = good ? s[r][i].z : res[i].y;
        vo[i] = good ? OOB : vo[i];
        any = any | (vo[i] != OOB);
        asm volatile("" : "+v"(res[i]), "+v"(vo[i]));
      }
      if (!__any(any)) return;
      ldset<N, STRIDE>(s[r], ws, vo);
    }
  }
}
template <int R, int N> DEV void keep(v4u (&s)[R][N]) {
  asm volatile("s_waitcnt vmcnt(0)" ::: "memory");
#pragma unroll
  for (int r = 0; r < R; ++r)
#pragma unroll
    for (int i = 0; i < N; ++i) asm volatile("" :: "v"(s[r][i]));
}
template <int SLEEP> DEV u32 gather1(rsrc_t ws, u32 voff, u32 tag) {
  v2u r;
#pragma nounroll
  for (u32 it = 0; it < SPIN_G; ++it) {
    r = __builtin_amdgcn_raw_buffer_load_b64(ws, voff, 0, 16);
    if (__all(r.y == tag)) break;
    CBAR();
    __builtin_amdgcn_s_sleep(SLEEP);
  }
  return r.x;
}
DEV void put2(rsrc_t ws, u32 voff, u32 a, u32 b, u32 tag) { __builtin_amdgcn_raw_buffer_store_b128(v4u{a, tag, b, tag}, ws, voff, 0, 16); }

#define TS(i) do { } while (0)

DEV u32 get_tag(LAS char* s) {
  v2u v;
#pragma nounroll
  for (u32 i = 0; i < SPIN_L; ++i) {
    v = *(volatile LAS v2u*)(s + C_TAG);
    if (__builtin_amdgcn_readfirstlane(v.x)) break;
    __builtin_amdgcn_s_sleep(1);
  }
  CBAR();
  return __builtin_amdgcn_readfirstlane(v.y);
}

DEV void load64(rsrc_t w, u32 stride, u32 soff, int lane, v4u (&r)[16]) {
  const u32 voff = (u32)(lane >> 3) * 8u * stride + (u32)(lane & 7) * 16u;
#pragma unroll
  for (int j = 0; j < 16; ++j) r[j] = __builtin_amdgcn_raw_buffer_load_b128(w, voff, soff + (u32)(64 * (j >> 3) + (j & 7)) * stride, W_AUX);
  CBAR();
}
DEV void load128(rsrc_t w, u32 stride, u32 soff, int lane, v4u (&r)[16]) {
  const u32 voff = (u32)(lane >> 4) * 8u * stride + (u32)(lane & 15) * 16u;
#pragma unroll
  for (int j = 0; j < 16; ++j) r[j] = __builtin_amdgcn_raw_buffer_load_b128(w, voff, soff + (u32)(32 * (j >> 3) + (j & 7)) * stride, W_AUX);
  CBAR();
}
DEV void perm16(const v4u (&r)[16], v4u (&f)[16]) {
#pragma unroll
  for (int p = 0; p < 2; ++p)
#pragma unroll
    for (int d = 0; d < 4; ++d)
#pragma unroll
      for (int i = 0; i < 4; ++i) {
        f[8 * p + 2 * d][i] = perm_lo(r[8 * p + 2 * i][d], r[8 * p + 2 * i + 1][d]);
        f[8 * p + 2 * d + 1][i] = perm_hi(r[8 * p + 2 * i][d], r[8 * p + 2 * i + 1][d]);
      }
#pragma unroll
  for (int i = 0; i < 16; ++i) asm volatile("" : "+v"(f[i]));
}
DEV v4f mma64(v4u a0, v4u a1, const v4u (&f)[16], bool lowh) {
  v4f c[8];
#pragma unroll
  for (int i = 0; i < 8; ++i) c[i] = mfma(a0, f[i], v4f{0.f, 0.f, 0.f, 0.f});
#pragma unroll
  for (int i = 0; i < 8; ++i) c[i] = mfma(a1, f[8 + i], c[i]);
  v4f y;
#pragma unroll
  for (int i = 0; i < 4; ++i) y[i] = (lowh ? c[i][0] : c[4 + i][1]) + dppf<0x128>(lowh ? c[4 + i][0] : c[i][1]);
  return y;
}
DEV void mma128(u32 lo, u32 hi, const v4u (&f)[16], float (&y)[8]) {
  const v4u a0 = {dppu<0x00>(lo), dppu<0x00>(hi), dppu<0xAA>(lo), dppu<0xAA>(hi)};
  const v4u a1 = {dppu<0x55>(lo), dppu<0x55>(hi), dppu<0xFF>(lo), dppu<0xFF>(hi)};
  v4f c[8];
#pragma unroll
  for (int i = 0; i < 8; ++i) c[i] = mfma(a0, f[i], v4f{0.f, 0.f, 0.f, 0.f});
#pragma unroll
  for (int i = 0; i < 8; ++i) { c[i] = mfma(a1, f[8 + i], c[i]); }
#pragma unroll
  for (int i = 0; i < 8; ++i) y[i] = c[i][0];
}
DEV float quadg_sum(float v) { v += dppf<0xB1>(v); v += dppf<0x4E>(v); return sum32(sum16(v)); }

template <int C> DEV void dma_k(u32 voff, const char* sbase, u32 ldst) {
  u32 keep;
  asm volatile("s_mov_b32 %0, m0\n\ts_mov_b32 m0, %3\n\ts_nop 0\n\tglobal_load_lds_dwordx4 %1, %2 offset:%4 nt\n\ts_mov_b32 m0, %0"
               : "=&s"(keep) : "v"(voff), "s"(sbase), "s"(ldst), "i"(C * 1024) : "memory");
}
#define WAITVM(n) asm volatile("s_waitcnt vmcnt(%0)" :: "i"(n) : "memory")
#define FU(x) __float_as_uint(x)
#define UF(x) __uint_as_float(x)

__global__ void __launch_bounds__(1024, 4)
fused_decode_kernel(const char* __restrict__ px, const char* __restrict__ pk, const char* __restrict__ pv,
                    const char* __restrict__ pwq, const char* __restrict__ pwo, const char* __restrict__ pwg,
                    const char* __restrict__ pwu, const char* __restrict__ pwd, const char* __restrict__ pc,
                    const char* __restrict__ pwm, char* pout, char* pws) {
  __shared__ __attribute__((aligned(16))) char smem_[LDS_BYTES];
  LAS char* sm = (LAS char*)smem_;
  const int lane = threadIdx.x & 63;
  const int wave = (__builtin_amdgcn_readfirstlane(threadIdx.x >> 6) + ROT) & 15;
  const int bx = blockIdx.x & 7, by = blockIdx.x >> 3;
  const int b = by >> 3, sp = by & 7;
  const int gi = b * 8 + bx;
  const int g = lane >> 4, n = lane & 15;
  const int qb = n >> 2, qs = n & 3;
  const bool isQ = sp == 1 || sp == 2, isMOD = sp == 0 || (sp == 5 && b < 2), isWO = sp == 3 || sp == 4, isFFN = sp == 0 && gi < 28;
  const int nun = (UNITS >> (4 * sp)) & 15;
  TS(0);
  asm volatile("" : "+s"(px), "+s"(pk), "+s"(pv), "+s"(pwq), "+s"(pwo), "+s"(pwg), "+s"(pwu), "+s"(pwd), "+s"(pc), "+s"(pwm), "+s"(pout), "+s"(pws));
  const rsrc_t ws = mkrsrc(pws, WS_END);

  if (wave < nun) {
    const int unit = (int)((unit_bases() >> (8 * sp)) & 255) + wave;
    const size_t rowoff = ((size_t)(gi * 4096 + unit * 64)) * 256;
    const char* kb = pk + rowoff;
    const rsrc_t vr = mkrsrc(pv + rowoff, 16384);
    const u32 lk = (u32)(uintptr_t)sm + (u32)wave * 16384u;
    u32 kvo[4];
#pragma unroll
    for (int c = 0; c < 4; ++c) kvo[c] = (u32)g * 256u + (u32)((n ^ (4 * g + c)) * 16);
    v4u vv[16];
    __builtin_amdgcn_s_barrier();
    if (DELAY && !(isQ | isMOD)) {
#pragma nounroll
      for (int i = 0; i < DELAY; ++i) __builtin_amdgcn_s_sleep(4);
    }
    TS(1);
#define KSTEP(i)                                                                         \
    { if (WIN && (i) >= WIN) WAITVM(WIN ? WIN - 1 : 0);                                   \
      if ((i) == KBQ && isQ) lds_wait<2>(sm, C_GO, 1);                                   \
      if ((i) == KBM && sp == 0) lds_wait<2>(sm, C_GO, 1);                               \
      dma_k<(i) & 3>(kvo[(i) & 3], kb + ((i) >> 2) * 4096, lk + ((i) >> 2) * 4096); }
    KSTEP(0) KSTEP(1) KSTEP(2) KSTEP(3) KSTEP(4) KSTEP(5) KSTEP(6) KSTEP(7)
    KSTEP(8) KSTEP(9) KSTEP(10) KSTEP(11) KSTEP(12) KSTEP(13) KSTEP(14) KSTEP(15)
    TS(11);
    lds_add(sm, C_KI, lane, 1u);
    lds_wait<1>(sm, C_KI, nun);
#pragma unroll
    for (int j = 0; j < 16; ++j) {
      if (WIN) WAITVM(WIN ? WIN - 1 : 0);
      if (j == KSIG) { WAITVM(KSIG); lds_add(sm, C_K, lane, 1u); TS(10); }
      vv[j] = __builtin_amdgcn_raw_buffer_load_b128(vr, (u32)lane * 16u, j * 1024, KV_NT ? 2 : 0);
      __builtin_amdgcn_sched_barrier(0);
      CBAR();
    }
    lds_add(sm, C_ISS, lane, 1u);
    TS(2);
    const u32 tag = get_tag(sm);
    lds_wait<1>(sm, C_LS, 3);
    TS(3);
    v4u pm[2][4];
#pragma unroll
    for (int u = 0; u < 2; ++u) {
      const v4u pa = *lp<v4u>(sm, L_P + (u32)(wave * 512 + u * 256 + g * 64 + qb * 16));
#pragma unroll
      for (int t = 0; t < 4; ++t) { pm[u][t] = qs == t ? pa : v4u{0u, 0u, 0u, 0u}; asm volatile("" : "+v"(pm[u][t])); }
    }
    v4f oa[2] = {{0.f, 0.f, 0.f, 0.f}, {0.f, 0.f, 0.f, 0.f}};
#pragma unroll
    for (int u = 0; u < 2; ++u) {
#pragma unroll
      for (int d = 0; d < 4; ++d) {
        v4u blo, bhi;
#pragma unroll
        for (int i = 0; i < 4; ++i) {
          blo[i] = perm_lo(vv[8 * u + 2 * i][d], vv[8 * u + 2 * i + 1][d]);
          bhi[i] = perm_hi(vv[8 * u + 2 * i][d], vv[8 * u + 2 * i + 1][d]);
        }
        oa[d >> 1] = mfma(pm[u][(2 * d) & 3], blo, oa[d >> 1]);
        oa[d >> 1] = mfma(pm[u][(2 * d + 1) & 3], bhi, oa[d >> 1]);
      }
    }
    *lp<v4f>(sm, (u32)wave * 16384u + (u32)lane * 32u) = oa[0];
    *lp<v4f>(sm, (u32)wave * 16384u + (u32)lane * 32u + 16) = oa[1];
    lds_add(sm, C_O, lane, 1u);
    TS(5);
    if (wave < 4) {
      lds_wait<0>(sm, C_O, nun);
      TS(6);
      v2f op[9];
#pragma unroll
      for (int w = 0; w < 9; ++w) op[w] = *lp<v2f>(sm, w * 16384 + (u32)wave * 512u + (u32)lane * 8u);
#pragma unroll
      for (int w = 4; w < 9; ++w) op[w] = w < nun ? op[w] : v2f{0.f, 0.f};
      const v2f o = (((op[0] + op[1]) + (op[2] + op[3])) + ((op[4] + op[5]) + (op[6] + op[7]))) + op[8];
      put2(ws, WS_OP + (u32)(((((bx * 4 + wave) * 4 + b) * 2) * 8 + sp) * 512) + (u32)(lane >> 5) * 4096u + (u32)(lane & 31) * 16u, FU(o[0]), FU(o[1]), tag);
      TS(7);
    }
    if (isWO) {
      if (wave < 8) {
        __builtin_amdgcn_s_setprio(PRIO);
        lds_wait<0>(sm, C_W, 1);
        float wgt[4];
#pragma unroll
        for (int i = 0; i < 4; ++i) wgt[i] = *lp<float>(sm, L_WS + (u32)((wave >> 1) * 32 + i * 8 + (lane >> 5) * 4));
        v4u og[4];
        gather2<4, 1024, 0>(ws, WS_OP + (u32)(((bx * 4 + b) * 8 + wave) * 4096 + lane * 16), tag, og);
        TS(8);
        float o0 = (wgt[0] * UF(og[0].x) + wgt[1] * UF(og[1].x)) + (wgt[2] * UF(og[2].x) + wgt[3] * UF(og[3].x));
        float o1 = (wgt[0] * UF(og[0].z) + wgt[1] * UF(og[1].z)) + (wgt[2] * UF(og[2].z) + wgt[3] * UF(og[3].z));
        o0 = sum32(o0); o1 = sum32(o1);
        *lp<u32>(sm, L_ACT + (u32)((wave >> 1) * 1088 + (wave & 1) * 128) + (u32)(lane & 31) * 4u) = pk2(o0, o1);
        lds_add(sm, C_A, lane, 1u);
        TS(9);
      }
    } else if (isFFN && wave < 4) {
      __builtin_amdgcn_s_setprio(PRIO);
      const int v = wave;
      v4u wf[16];
      const u32 p0 = (u32)((lane >> 3) * 32 + ((lane >> 1) & 3) * 8 + (lane & 1) * 2);
      v4u sb[2], cb[2];
      {
        v4u wr[16];
        load128(mkrsrc(pwd, 1792 * 512 * 2), 1024, (u32)(gi * 64 * 1024 + v * 256), lane, wr);
        gather2<2, 32, 8>(ws, WS_MG + (u32)(v * 12288 + 6144) + p0 * 8u, tag, sb);
        gather2<2, 32, 8>(ws, WS_MG + (u32)(v * 12288 + 8192) + p0 * 8u, tag, cb);
        perm16(wr, wf);
      }
      float cb1[8];
#pragma unroll
      for (int i = 0; i < 2; ++i) {
        cb1[4 * i] = rb(1.f + bflo(cb[i].x)); cb1[4 * i + 1] = rb(1.f + bfhi(cb[i].x));
        cb1[4 * i + 2] = rb(1.f + bflo(cb[i].z)); cb1[4 * i + 3] = rb(1.f + bfhi(cb[i].z));
      }
#pragma unroll
      for (int i = 0; i < 8; ++i) asm volatile("" : "+v"(cb1[i]));
      TS(4);
      v4u xg[2];
      gather2<2, 64, XSLEEP>(ws, WS_XG + (u32)(bx * 8192 + v * 2048 + (lane >> 3) * 256 + (lane & 1) * 128 + ((lane >> 1) & 3) * 16), tag, xg);
      TS(8);
      const float xf[8] = {bflo(xg[0].x), bfhi(xg[0].x), bflo(xg[1].x), bfhi(xg[1].x), bflo(xg[0].z), bfhi(xg[0].z), bflo(xg[1].z), bfhi(xg[1].z)};
      const float piv = bflo(__builtin_amdgcn_readfirstlane(xg[0].x));
      float dd[8];
#pragma unroll
      for (int i = 0; i < 8; ++i) dd[i] = xf[i] - piv;
      float s1 = ((dd[0] + dd[1]) + (dd[2] + dd[3])) + ((dd[4] + dd[5]) + (dd[6] + dd[7]));
      float s2 = ((dd[0] * dd[0] + dd[1] * dd[1]) + (dd[2] * dd[2] + dd[3] * dd[3])) + ((dd[4] * dd[4] + dd[5] * dd[5]) + (dd[6] * dd[6] + dd[7] * dd[7]));
      s1 = sum32(sum16(rowsum(s1))); s2 = sum32(sum16(rowsum(s2)));
      const float md = s1 * (1.f / 512.f);
      const float var = rb(fmaxf(s2 * (1.f / 512.f) - md * md, 0.f));
      const float m = rb(piv + md);
      const float rs = __builtin_amdgcn_rcpf(rb(__builtin_amdgcn_sqrtf(rb(var + 1e-5f))));
#pragma unroll
      for (int i = 0; i < 2; ++i) {
        v2u hv;
        hv.x = pk2(rb(rb(rb(xf[4 * i] - m) * rs) * cb1[4 * i]) + bflo(sb[i].x), rb(rb(rb(xf[4 * i + 1] - m) * rs) * cb1[4 * i + 1]) + bfhi(sb[i].x));
        hv.y = pk2(rb(rb(rb(xf[4 * i + 2] - m) * rs) * cb1[4 * i + 2]) + bflo(sb[i].z), rb(rb(rb(xf[4 * i + 3] - m) * rs) * cb1[4 * i + 3]) + bfhi(sb[i].z));
        *lp<v2u>(sm, L_ACT + (u32)(v * 1088 + i * 16) + p0 * 4u) = hv;
      }
      lds_add(sm, C_A, lane, 1u);
      TS(9);
      lds_wait<0>(sm, C_F, 8);
      TS(10);
      v4f p[8];
#pragma unroll
      for (int i = 0; i < 8; ++i) p[i] = *lp<v4f>(sm, L_BIG + (u32)(i * 1024 + qb * 256 + (qs & 1) * 128 + g * 32 + (qs >> 1) * 16));
      const v4f ga = (p[0] + p[1]) + (p[2] + p[3]), up = (p[4] + p[5]) + (p[6] + p[7]);
      const u32 lo = pk2(rb(silu(rb(ga[0]))) * rb(up[0]), rb(silu(rb(ga[1]))) * rb(up[1]));
      const u32 hi = pk2(rb(silu(rb(ga[2]))) * rb(up[2]), rb(silu(rb(ga[3]))) * rb(up[3]));
      float y[8];
      mma128(lo, hi, wf, y);
      const u32 o = WS_FP + (u32)((((g * 4 + v) * 2 + (n >> 3)) * 4) * 4096 + gi * 128 + (n & 7) * 16);
#pragma unroll
      for (int i = 0; i < 4; ++i) put2(ws, o + i * 4096, FU(y[2 * i]), FU(y[2 * i + 1]), tag);
      TS(11);
    }
  } else if (wave >= 13) {
    __builtin_amdgcn_s_setprio(PRIO);
    const int j = wave - 13;
    u32 tag = 0u;
    if (j == 2) tag = *(const volatile CAS u32*)(pws + WS_EPOCH);
    if (j == 1) {
      if (lane < 16) *lp<u32>(sm, L_CTL + lane * 4) = 0u;
      asm volatile("s_waitcnt lgkmcnt(0)" ::: "memory");
    }
    __builtin_amdgcn_s_barrier();
    if (j == 2) {
      tag += 1u;
      *(volatile LAS v2u*)(sm + C_TAG) = v2u{1u, tag};
      CBAR();
    } else {
      tag = get_tag(sm);
    }
    TS(1);
    lds_wait<4>(sm, C_K, nun);
    TS(2);
    const u32 krow = (u32)j * 49152u + (u32)(4 * qs + qb) * 256u;
    u32 ko[4];
#pragma unroll
    for (int ks = 0; ks < 4; ++ks) ko[ks] = krow + (u32)(((4 * ks + g) ^ n) * 16);
    v4u kf[12];
#pragma unroll
    for (int i = 0; i < 12; ++i) kf[i] = *lp<v4u>(sm, ko[i / 3] + (i % 3) * 16384);
    lds_wait<2>(sm, C_QRDY, 1);
    TS(3);
    v4u qf[4];
#pragma unroll
    for (int ks = 0; ks < 4; ++ks) qf[ks] = *lp<v4u>(sm, L_Q + (u32)qb * 320u + ks * 64 + g * 16);
    v4f sc[3];
#pragma unroll
    for (int w = 0; w < 3; ++w) sc[w] = v4f{0.f, 0.f, 0.f, 0.f};
#pragma unroll
    for (int t = 0; t < 4; ++t) {
#pragma unroll
      for (int ks = 0; ks < 4; ++ks) {
        const v4u qm = qs == t ? qf[ks] : v4u{0u, 0u, 0u, 0u};
#pragma unroll
        for (int w = 0; w < 3; ++w) {
          sc[w] = mfma(kf[3 * ks + w], qm, sc[w]);
          if (t < 3) kf[3 * ks + w] = *lp<v4u>(sm, ko[ks] + w * 16384 + (t + 1) * 4096);
        }
        __builtin_amdgcn_sched_barrier(0);
      }
    }
    TS(4);
    float mx = -__builtin_inff();
#pragma unroll
    for (int w = 0; w < 3; ++w) {
      const bool valid = 3 * j + w < nun;
#pragma unroll
      for (int r = 0; r < 4; ++r) { sc[w][r] = valid ? rb(rb(sc[w][r]) * 0.08838834764831845f) : -__builtin_inff(); mx = fmaxf(mx, sc[w][r]); }
    }
    mx = fmaxf(mx, dppf<0xB1>(mx)); mx = fmaxf(mx, dppf<0x4E>(mx));
    mx = max32(max16(mx));
    *lp<float>(sm, L_MX + (u32)qb * 16u + j * 4) = mx;
    lds_add(sm, C_MX, lane, 1u);
    lds_wait<0>(sm, C_MX, 3);
    const v4f m0 = *lp<v4f>(sm, L_MX + (u32)qb * 16u);
    mx = fmaxf(fmaxf(m0[0], m0[1]), m0[2]);
    TS(5);
    float ls = 0.f;
#pragma unroll
    for (int w = 0; w < 3; ++w) {
#pragma unroll
      for (int r = 0; r < 4; ++r) sc[w][r] = __expf(sc[w][r] - mx);
      ls += (sc[w][0] + sc[w][1]) + (sc[w][2] + sc[w][3]);
      *lp<v2u>(sm, L_P + (u32)(j * 1536 + w * 512 + (qs >> 1) * 256 + g * 64 + qb * 16 + (qs & 1) * 8)) = v2u{pk2(sc[w][0], sc[w][1]), pk2(sc[w][2], sc[w][3])};
    }
    ls = quadg_sum(ls);
    *lp<float>(sm, L_LS + (u32)qb * 16u + j * 4) = ls;
    if (lds_inc_ret(sm, C_LS, lane) == 2u) {
      const v4f l0 = *lp<v4f>(sm, L_LS + (u32)qb * 16u);
      ls = (l0[0] + l0[1]) + l0[2];
      if ((lane & 0x33) == 0) put2(ws, WS_ML + (u32)((((bx * 4 + qb) * 4 + b) * 8 + sp) * 16), FU(mx), FU(ls), tag);
      TS(6);
      if (isWO) {
        v4u ml[1];
        gather2<1, 0, 8>(ws, WS_ML + (u32)(((bx * 4 + b) * 32 + (lane & 31)) * 16), tag, ml);
        const float ms = UF(ml[0].x);
        float M = fmaxf(ms, dppf<0xB1>(ms)); M = fmaxf(M, dppf<0x4E>(M)); M = fmaxf(M, dppf<0x141>(M));
        const float e = __expf(ms - M);
        float L = e * UF(ml[0].z);
        L += dppf<0xB1>(L); L += dppf<0x4E>(L); L += dppf<0x141>(L);
        *lp<float>(sm, L_WS + (u32)(lane & 31) * 4u) = e * __builtin_amdgcn_rcpf(L);
        lds_set(sm, C_W);
        TS(7);
      }
    }
  } else {
    __builtin_amdgcn_s_setprio(PRIO);
    const u64 tstart = __builtin_amdgcn_s_memrealtime();
    const int v = (wave - 1) & 3;
    const bool main4 = wave >= 9;
    const u32 ocol = (u32)((n & 7) * 32 + (n >> 3) * 16);
    if (main4 && (isQ | isMOD)) {
      const int cgp = sp == 0 ? gi : 32 + gi;
      const u32 k0 = (u32)(v * 128 + (qs >> 1) * 64 + g * 16 + (qs & 1) * 8);
      v4u own = *(const GAS v4u*)((isQ ? px : pc) + qb * 1024 + k0 * 2);
      v4u xv[4];
      if (isQ) {
#pragma unroll
        for (int i = 0; i < 4; ++i) xv[i] = *(const GAS v4u*)(px + qb * 1024 + (qs + 4 * g) * 64 + i * 16);
      }
      CBAR();
      v4u wf[16];
      float xn[8];
      {
        v4u wr[16];
        const u32 stride = isQ ? 8192u : 6144u;
        load64(mkrsrc(isQ ? pwq : pwm, 512 * stride), stride, (u32)v * 128u * stride + (u32)(isQ ? (bx * 4 + b) * 256 + (sp - 1) * 128 : cgp * 128), lane, wr);
        __builtin_amdgcn_s_barrier();
        TS(1);
        if (isQ) {
          float s = 0.f;
#pragma unroll
          for (int i = 0; i < 4; ++i)
#pragma unroll
            for (int k = 0; k < 4; ++k) s += bflo(xv[i][k]) + bfhi(xv[i][k]);
          const float mf = quadg_sum(s) * (1.f / 512.f);
          float q = 0.f;
#pragma unroll
          for (int i = 0; i < 4; ++i)
#pragma unroll
            for (int k = 0; k < 4; ++k) { float d0 = bflo(xv[i][k]) - mf, d1 = bfhi(xv[i][k]) - mf; q += d0 * d0 + d1 * d1; }
          const float var = rb(quadg_sum(q) * (1.f / 512.f));
          const float rs = __builtin_amdgcn_rcpf(rb(__builtin_amdgcn_sqrtf(rb(var + 1e-5f))));
          const float m = rb(mf);
#pragma unroll
          for (int i = 0; i < 4; ++i) own[i] = pk2(rb(bflo(own[i]) - m) * rs, rb(bfhi(own[i]) - m) * rs);
        } else {
#pragma unroll
          for (int i = 0; i < 4; ++i) own[i] = pk2(silu(bflo(own[i])), silu(bfhi(own[i])));
        }
        asm volatile("" : "+v"(own));
        perm16(wr, wf);
      }
      const u32 tag = get_tag(sm);
      TS(2);
      if (isQ) {
        v4u sa[2], ca[2];
        {
          v4u mg[4];
          const u32 mo = WS_MG + (u32)(qb * 12288) + k0 * 4u;
#pragma unroll
          for (int i = 0; i < 4; ++i) mg[i] = __builtin_amdgcn_raw_buffer_load_b128(ws, mo, (i & 1) * 2048 + (i >> 1) * 16, 16);
#pragma nounroll
          for (u32 it = 0; it < SPIN_G; ++it) {
            bool bad[4], any = false;
#pragma unroll
            for (int i = 0; i < 4; ++i) { bad[i] = (mg[i].y != tag) | (mg[i].w != tag); any = any | bad[i]; }
            if (!__any(any)) break;
            CBAR();
            if (HSLEEP) __builtin_amdgcn_s_sleep(HSLEEP);
#pragma unroll
            for (int i = 0; i < 4; ++i)
              if (bad[i]) mg[i] = __builtin_amdgcn_raw_buffer_load_b128(ws, mo, (i & 1) * 2048 + (i >> 1) * 16, 16);
          }
          sa[0] = mg[0]; ca[0] = mg[1]; sa[1] = mg[2]; ca[1] = mg[3];
        }
#pragma unroll
        for (int i = 0; i < 4; ++i) {
          const u32 s2 = (i & 1) ? sa[i >> 1].z : sa[i >> 1].x, c2 = (i & 1) ? ca[i >> 1].z : ca[i >> 1].x;
          own[i] = pk2(rb(bflo(own[i]) * rb(1.f + bflo(c2))) + bflo(s2), rb(bfhi(own[i]) * rb(1.f + bfhi(c2))) + bfhi(s2));
        }
      }
      TS(3);
      v4u a0, a1;
#pragma unroll
      for (int i = 0; i < 4; ++i) { a0[i] = dppu<0x44>(own[i]); a1[i] = dppu<0xEE>(own[i]); }
      const v4f r = mma64(a0, a1, wf, n < 8);
      *lp<v4f>(sm, L_PART + (u32)(v * 1024 + lane * 16)) = r;
      if (lds_inc_ret(sm, C_P, lane) == 3) {
        v4f p[4];
#pragma unroll
        for (int i = 0; i < 4; ++i) p[i] = *lp<v4f>(sm, L_PART + (u32)(i * 1024 + lane * 16));
        const v4f a = (p[0] + p[1]) + (p[2] + p[3]);
        const u32 dst = isQ ? WS_QG + (u32)(g * 16384 + (bx * 4 + b) * 512 + (sp - 1) * 256) : WS_MG + (u32)(g * 12288 + cgp * 256);
        put2(ws, dst + ocol, pk2(a[0], a[1]), pk2(a[2], a[3]), tag);
        lds_set(sm, C_GO);
      }
      TS(4);
    } else {
      __builtin_amdgcn_s_barrier();
    }
    const u32 tag = get_tag(sm);
    if (wave == 12) {
#pragma nounroll
      for (u32 i = 0; i < SPIN_L && __builtin_amdgcn_s_memrealtime() - tstart < QT0; ++i) __builtin_amdgcn_s_sleep(4);
      v2u qg[2];
      v4u qsets[QR][2];
      const u32 qo = WS_QG + (u32)(b * 16384 + bx * 2048 + lane * 16);
      gatherp<2, 1024, QR, QGAP>(ws, qo, qo, tag, qg, qsets);
      *lp<v2u>(sm, L_Q + (u32)(lane >> 5) * 320u + (u32)(lane & 31) * 8u) = qg[0];
      *lp<v2u>(sm, L_Q + 640 + (u32)(lane >> 5) * 320u + (u32)(lane & 31) * 8u) = qg[1];
      lds_set(sm, C_QRDY);
      TS(11);
      keep(qsets);
    }
    if (main4 ? (isWO | isFFN) : (isFFN && wave >= 5)) {
      const int head = bx * 4 + b, hf = sp - 3;
      if (GU_TRIG && isFFN) lds_wait<4>(sm, C_O, nun); else lds_wait<4>(sm, C_ISS, nun);
#pragma nounroll
      for (u32 i = 0; i < SPIN_L && __builtin_amdgcn_s_memrealtime() - tstart < (isWO ? WOT0 : GUT0); ++i) __builtin_amdgcn_s_sleep(4);
      TS(5);
      v4u wf[16];
      {
        v4u wr[16];
        const u32 stride = isWO ? 1024u : 3584u;
        load64(mkrsrc(isWO ? pwo : main4 ? pwu : pwg, isWO ? 4096 * 1024 : 512 * 3584), stride,
               (u32)(isWO ? head * 128 * 1024 + hf * 512 + v * 128 : v * 128 * 3584 + gi * 128), lane, wr);
        perm16(wr, wf);
      }
      const u32 ao = L_ACT + (u32)(qb * 1088 + g * 32 + (qs & 1) * 16 + (isWO ? 0 : v * 256));
      TS(6);
      lds_wait<2>(sm, C_A, 1);
      lds_wait<0>(sm, C_A, isWO ? 8 : 4);
      TS(9);
      const v4u a0 = *lp<v4u>(sm, ao), a1 = *lp<v4u>(sm, ao + 128);
      const v4f r = mma64(a0, a1, wf, n < 8);
      if (isWO) {
        const u32 o = WS_WP + (u32)(((((g * 2 + hf) * 4 + v) * 2 + (n >> 3)) * 2) * 4096 + head * 128 + (n & 7) * 16);
        put2(ws, o, FU(r[0]), FU(r[1]), tag);
        put2(ws, o + 4096, FU(r[2]), FU(r[3]), tag);
      } else {
        *lp<v4f>(sm, L_BIG + (u32)((wave - 5) * 1024 + g * 256) + ocol) = r;
        lds_add(sm, C_F, lane, 1u);
      }
      TS(10);
    } else if (main4 && sp == 6) {
      const int task = gi * 4 + v, tb = task >> 5;
      const u32 pr = (u32)(((task >> 2) & 7) * 32 + (lane & 7) * 4 + (task & 3));
      const u32 xp = *(const GAS u32*)(px + tb * 1024 + pr * 4);
      lds_wait<4>(sm, C_O, nun);
      const u32 ga = gather1<8>(ws, WS_MG + (u32)(tb * 12288 + 4096) + pr * 8u, tag);
      const u32 gb = gather1<8>(ws, WS_MG + (u32)(tb * 12288 + 10240) + pr * 8u, tag);
      TS(5);
      v4u pg[4];
      gather2<4, 1024, 0>(ws, WS_WP + (u32)(task * 4096 + lane * 16), tag, pg);
      TS(6);
      float a0 = (UF(pg[0].x) + UF(pg[1].x)) + (UF(pg[2].x) + UF(pg[3].x));
      float a1 = (UF(pg[0].z) + UF(pg[1].z)) + (UF(pg[2].z) + UF(pg[3].z));
      a0 += dppf<0x128>(a0); a1 += dppf<0x128>(a1);
      a0 = sum32(sum16(a0)); a1 = sum32(sum16(a1));
      const u32 x1 = pk2(bflo(xp) + rb(bflo(ga) * rb(a0)), bfhi(xp) + rb(bfhi(ga) * rb(a1)));
      const u32 x1n = dppu<0xB1>(x1);
      if (!(lane & 1)) put2(ws, WS_XG + (u32)((lane >> 3) * 8192 + task * 64 + (lane & 7) * 8), x1, x1n, tag);
      TS(7);
      gather2<4, 1024, 0>(ws, WS_FP + (u32)(task * 4096 + lane * 16), tag, pg, WS_FP + (u32)(task * 4096 + (lane & 31) * 16));
      TS(8);
      const bool lw = lane < 32;
      a0 = (UF(pg[0].x) + UF(pg[1].x)) + (UF(pg[2].x) + (lw ? UF(pg[3].x) : 0.f));
      a1 = (UF(pg[0].z) + UF(pg[1].z)) + (UF(pg[2].z) + (lw ? UF(pg[3].z) : 0.f));
      a0 += dppf<0x128>(a0); a1 += dppf<0x128>(a1);
      a0 = sum32(sum16(a0)); a1 = sum32(sum16(a1));
      const u32 o = pk2(bflo(x1) + rb(bflo(gb) * rb(a0)), bfhi(x1) + rb(bfhi(gb) * rb(a1)));
      if (lane < 8) *(GAS u32*)(pout + tb * 1024 + pr * 4) = o;
      if (task == 0 && lane == 0) __builtin_amdgcn_raw_buffer_store_b32(tag, ws, WS_EPOCH, 0, 16);
      TS(9);
    }
  }
}

extern "C" void kernel_launch(void* const* d_in, const int* in_sizes, int n_in,
                              void* d_out, int out_size, void* d_ws, size_t ws_size,
                              hipStream_t stream) {
  fused_decode_kernel<<<256, 1024, 0, stream>>>(
      (const char*)d_in[0], (const char*)d_in[1], (const char*)d_in[2], (const char*)d_in[3], (const char*)d_in[4],
      (const char*)d_in[6], (const char*)d_in[7], (const char*)d_in[8], (const char*)d_in[9], (const char*)d_in[10],
      (char*)d_out, (char*)d_ws);
}
```

```cpp
#include <hip/hip_runtime.h>
#include <stdint.h>

typedef unsigned int u32;
typedef unsigned long long u64;
typedef u32 v4u __attribute__((ext_vector_type(4)));
typedef u32 v2u __attribute__((ext_vector_type(2)));
typedef float v4f __attribute__((ext_vector_type(4)));
typedef float v2f __attribute__((ext_vector_type(2)));
typedef __bf16 v8bf __attribute__((ext_vector_type(8)));
typedef __bf16 v2bf __attribute__((ext_vector_type(2)));
typedef __amdgpu_buffer_rsrc_t rsrc_t;
#define LAS __attribute__((address_space(3)))
#define CAS __attribute__((address_space(4)))
#define GAS __attribute__((address_space(1)))
#define DEV __device__ __forceinline__
#define CBAR() asm volatile("" ::: "memory")

#define UNITS 0x99988669u
#define WIN 0
#define KSIG 4
#define KSIGQ 4
#define KB1 0
#define KBQ 16
#define PFT 1
#define SBT0 700
#define PT0 100
#define PT1 1150
#define PQ0 240
#define PW0 240
#define PW1 (PT1 - 100)
#define ROTK 1
#define RT0 200
#define OT0 (PT1 + OD)
#define GT0 (PT1 + 100)
#define FT0 (PT1 + 100)
#define XSW 4
#define MT1 (PT1 - 30)
#define OD 210
#define KPRE 4
#define KV_NT 1
#define W_AUX 0
#define PRIO 3
#define DELAY 4
#define QT0 720
#define QR 1
#define QGAP 16
#define HSLEEP 2
#define XSLEEP 4
#define GU_TRIG 1
#define WOT0 900
#define GUT0 0
#define ROT 9
#define GO_EARLY 0
#define WO_TRIG 0
#define QBAR 1
#define MT0 150
#define PFQ 2
#define PF_AUX 0
#define MLATE 0
#define HT0 400
#define WSL 4
#define SLOT 64
#define PH_B 118
#define PH_X 200
#define PH_F 388

constexpr int unit_count(int s) { return (UNITS >> (4 * s)) & 15; }
constexpr int unit_base(int s) { return s == 0 ? 0 : unit_base(s - 1) + unit_count(s - 1); }
constexpr u64 unit_bases() { u64 r = 0; for (int s = 0; s < 8; ++s) r |= (u64)unit_base(s) << (8 * s); return r; }
static_assert(unit_base(8) == 64 && unit_count(1) <= 7 && unit_count(2) <= 7 && unit_count(1) >= 5 && unit_count(2) >= 5, "unit table");
static_assert(unit_count(0) >= 8 && unit_count(3) >= 8 && unit_count(4) >= 8 && unit_count(5) >= 8 && unit_count(6) >= 5 && unit_count(7) >= 5, "unit table");

constexpr u32 WS_EPOCH = 0;
constexpr u32 WS_TG = 1024;
constexpr u32 WS_MG = 4096;
constexpr u32 WS_QG = WS_MG + 49152;
constexpr u32 WS_XG = WS_QG + 65536;
constexpr u32 WS_ML = WS_XG + 131072;
constexpr u32 WS_OP = WS_ML + 16384;
constexpr u32 WPS = 33 * 128, FPS = 57 * 128;
constexpr u32 WS_WP = WS_OP + 1048576;
constexpr u32 WS_FP = WS_WP + 128 * WPS;
constexpr u32 WS_END = WS_FP + 128 * FPS;
constexpr u32 OOB = 0x7ff00000u;

constexpr u32 L_BIG = 8 * 16384;
constexpr u32 L_Q = 147456;
constexpr u32 L_P = L_Q + 1280;
constexpr u32 L_ACT = L_P + 4608;
constexpr u32 L_PART = L_ACT + 4352;
constexpr u32 L_MX = L_PART + 4096;
constexpr u32 L_LS = L_MX + 64;
constexpr u32 L_WS = L_LS + 64;
constexpr u32 L_CTL = L_WS + 128;
constexpr u32 C_TAG = L_CTL, C_GO = L_CTL + 8, C_ISS = L_CTL + 12, C_QRDY = L_CTL + 16, C_K = L_CTL + 20, C_MX = L_CTL + 24,
              C_LS = L_CTL + 28, C_O = L_CTL + 32, C_A = L_CTL + 36, C_P = L_CTL + 40, C_W = L_CTL + 44, C_F = L_CTL + 48,
              C_KI = L_CTL + 52, C_WL = L_CTL + 56, C_M = L_CTL + 60, C_PUB = L_CTL + 64, C_W1 = L_CTL + 68, C_REF = L_CTL + 72;
constexpr u32 F_S = L_CTL + 96, F_A = L_CTL + 128, F_F = L_CTL + 160, F_O = L_CTL + 192;
constexpr u32 LDS_BYTES = L_CTL + 256;

constexpr u32 SPIN_G = 1u << 18;
constexpr u32 SPIN_L = 1u << 22;

DEV float bflo(u32 v) { return __uint_as_float(v << 16); }
DEV float bfhi(u32 v) { return __uint_as_float(v & 0xffff0000u); }
DEV u32 pk2(float a, float b) { v2f f = {a, b}; v2bf h = __builtin_convertvector(f, v2bf); return __builtin_bit_cast(u32, h); }
DEV float rb(float f) { return bflo(pk2(f, f)); }
DEV v4f mfma(v4u a, v4u b, v4f c) {
  return __builtin_amdgcn_mfma_f32_16x16x32_bf16(__builtin_bit_cast(v8bf, a), __builtin_bit_cast(v8bf, b), c, 0, 0, 0);
}
DEV u32 perm_lo(u32 even, u32 odd) { return __builtin_amdgcn_perm(odd, even, 0x05040100u); }
DEV u32 perm_hi(u32 even, u32 odd) { return __builtin_amdgcn_perm(odd, even, 0x07060302u); }
template <int CTRL> DEV u32 dppu(u32 v) { return __builtin_amdgcn_update_dpp(0u, v, CTRL, 0xf, 0xf, true); }
template <int CTRL> DEV float dppf(float v) { return __uint_as_float(dppu<CTRL>(__float_as_uint(v))); }
DEV float sum16(float v) { u32 x = __float_as_uint(v); auto r = __builtin_amdgcn_permlane16_swap(x, x, false, false); return __uint_as_float(r[0]) + __uint_as_float(r[1]); }
DEV float sum32(float v) { u32 x = __float_as_uint(v); auto r = __builtin_amdgcn_permlane32_swap(x, x, false, false); return __uint_as_float(r[0]) + __uint_as_float(r[1]); }
DEV float max16(float v) { u32 x = __float_as_uint(v); auto r = __builtin_amdgcn_permlane16_swap(x, x, false, false); return fmaxf(__uint_as_float(r[0]), __uint_as_float(r[1])); }
DEV float max32(float v) { u32 x = __float_as_uint(v); auto r = __builtin_amdgcn_permlane32_swap(x, x, false, false); return fmaxf(__uint_as_float(r[0]), __uint_as_float(r[1])); }
DEV float rowsum(float v) {
  v += dppf<0xB1>(v);
  v += dppf<0x4E>(v);
  v += dppf<0x141>(v);
  v += dppf<0x140>(v);
  return v;
}
DEV float silu(float v) { return v * __builtin_amdgcn_rcpf(1.f + __expf(-v)); }

DEV rsrc_t mkrsrc(const void* p, u32 bytes) { return __builtin_amdgcn_make_buffer_rsrc((void*)p, (short)0, (int)bytes, 0x00020000); }

template <typename T> DEV LAS T* lp(LAS char* s, u32 off) { return (LAS T*)(s + off); }

template <int SLEEP> DEV void lds_wait(LAS char* s, u32 off, u32 target) {
#pragma nounroll
  for (u32 i = 0; i < SPIN_L; ++i) {
    u32 v = __builtin_amdgcn_readfirstlane(*(volatile LAS u32*)(s + off));
    if (v >= target) break;
    if (SLEEP) __builtin_amdgcn_s_sleep(SLEEP);
  }
  CBAR();
}
DEV void lds_add(LAS char* s, u32 off, int lane, u32 v) {
  CBAR();
  if (lane == 0) __hip_atomic_fetch_add((LAS u32*)(s + off), v, __ATOMIC_RELAXED, __HIP_MEMORY_SCOPE_WORKGROUP);
  CBAR();
}
DEV u32 lds_inc_ret(LAS char* s, u32 off, int lane) {
  CBAR();
  u32 r = 0;
  if (lane == 0) r = __hip_atomic_fetch_add((LAS u32*)(s + off), 1u, __ATOMIC_RELAXED, __HIP_MEMORY_SCOPE_WORKGROUP);
  r = __builtin_amdgcn_readfirstlane(r);
  CBAR();
  return r;
}
DEV void lds_set(LAS char* s, u32 off) { CBAR(); *(volatile LAS u32*)(s + off) = 1u; CBAR(); }
template <int N, int STRIDE, int SLEEP> DEV void gather2(rsrc_t ws, u32 voff, u32 tag, v4u (&r)[N], u32 vlast) {
#pragma unroll
  for (int i = 0; i < N; ++i) r[i] = __builtin_amdgcn_raw_buffer_load_b128(ws, i == N - 1 ? vlast : voff, i * STRIDE, 16);
#pragma nounroll
  for (u32 it = 0; it < SPIN_G; ++it) {
    bool bad[N], any = false;
#pragma unroll
    for (int i = 0; i < N; ++i) { bad[i] = (r[i].y != tag) | (r[i].w != tag); any = any | bad[i]; }
    if (!__any(any)) break;
    CBAR();
    if (SLEEP) __builtin_amdgcn_s_sleep(SLEEP);
#pragma unroll
    for (int i = 0; i < N; ++i)
      if (bad[i]) r[i] = __builtin_amdgcn_raw_buffer_load_b128(ws, i == N - 1 ? vlast : voff, i * STRIDE, 16);
  }
}
template <int N, int STRIDE, int SLEEP> DEV void gather2(rsrc_t ws, u32 voff, u32 tag, v4u (&r)[N]) { gather2<N, STRIDE, SLEEP>(ws, voff, tag, r, voff); }
template <int OFF> DEV void ldg(v4u& r, rsrc_t ws, u32 vo) {
  asm volatile("buffer_load_dwordx4 %0, %1, %2, 0 offen offset:%3 sc1" : "+v"(r) : "v"(vo), "s"(ws), "i"(OFF) : "memory");
}
template <int N, int STRIDE> DEV void ldset(v4u (&s)[N], rsrc_t ws, const u32 (&vo)[N]) {
  ldg<0>(s[0], ws, vo[0]);
  if constexpr (N > 1) ldg<STRIDE>(s[1], ws, vo[1]);
  if constexpr (N > 2) ldg<2 * STRIDE>(s[2], ws, vo[2]);
  if constexpr (N > 3) ldg<3 * STRIDE>(s[3], ws, vo[3]);
}
template <int N> DEV u32 chkset(v4u (&s)[N], u32 tag, v2u (&res)[N], u32 (&vo)[N]) {
  u32 miss = 0;
#pragma unroll
  for (int i = 0; i < N; ++i) {
    asm volatile("" : "+v"(s[i]));
    const bool good = (vo[i] != OOB) & (s[i].y == tag) & (s[i].w == tag);
    res[i].x = good ? s[i].x : res[i].x; res[i].y = good ? s[i].z : res[i].y;
    vo[i] = good ? OOB : vo[i];
    miss += (u32)__builtin_popcountll(__builtin_amdgcn_ballot_w64(vo[i] != OOB));
    asm volatile("" : "+v"(res[i]), "+v"(vo[i]));
  }
  return miss;
}
template <int N, int STRIDE, int R, int GAP> DEV void gatherp(rsrc_t ws, u32 voff, u32 tag, v2u (&res)[N], v4u (&s)[R][N]) {
  u32 vo[N];
#pragma unroll
  for (int i = 0; i < N; ++i) { vo[i] = voff; res[i] = v2u{0u, 0u}; }
#pragma unroll
  for (int r = 0; r < R; ++r) {
#pragma unroll
    for (int i = 0; i < N; ++i) asm volatile("" : "=v"(s[r][i]));
    ldset<N, STRIDE>(s[r], ws, vo);
    if (r < R - 1) __builtin_amdgcn_s_sleep(GAP);
  }
#pragma nounroll
  for (u32 it = 0; it < SPIN_G; ++it) {
#pragma unroll
    for (int r = 0; r < R; ++r) {
      asm volatile("s_waitcnt vmcnt(%0)" :: "i"((R - 1) * N) : "memory");
      if (chkset<N>(s[r], tag, res, vo) == 0) return;
      ldset<N, STRIDE>(s[r], ws, vo);
    }
  }
}
DEV u32 now32() { return (u32)__builtin_amdgcn_s_memrealtime(); }
DEV void sleep_until(u32 t) {
#pragma nounroll
  for (u32 i = 0; i < 4096u && (int)(now32() - t) < 0; ++i) __builtin_amdgcn_s_sleep(2);
  CBAR();
}
DEV void slot_wait(u32 first) {
  const u32 t = now32(), d = first - t;
  const u32 end = t + ((int)d < 0 ? d % (u32)SLOT : d);
#pragma nounroll
  for (u32 i = 0; i < 4096u && (int)(now32() - end) < 0; ++i) { }
  CBAR();
}
DEV u32 tagbad(const v4u& r, u32 tag) { return __builtin_amdgcn_bitop3_b32(r.y, r.w, tag, 0x7e); }
DEV bool anynz(u32 v) { return __builtin_amdgcn_ballot_w64(v != 0u) != 0ull; }
#define PIN(x) asm volatile("" : "+v"(x))
template <int N> DEV void gathersum(rsrc_t ws, u32 voff, u32 tag, u32 first, float& a0, float& a1) {
  sleep_until(first - 12u);
  u32 left = SPIN_G, bad;
#pragma nounroll
  do {
    slot_wait(first);
    v4u r[N];
#pragma unroll
    for (int i = 0; i < N; ++i) r[i] = __builtin_amdgcn_raw_buffer_load_b128(ws, voff, i * 1024, 16);
    --left;
    const u32 lim = left ? ~0u : 0u;
    __builtin_amdgcn_sched_barrier(0);
    bad = tagbad(r[0], tag); a0 = __uint_as_float(r[0].x); a1 = __uint_as_float(r[0].z);
#pragma unroll
    for (int i = 1; i < N; ++i) {
      bad |= tagbad(r[i], tag); a0 += __uint_as_float(r[i].x); a1 += __uint_as_float(r[i].z);
      PIN(bad); PIN(a0); PIN(a1);
      __builtin_amdgcn_sched_barrier(0);
    }
    bad &= lim;
  } while (anynz(bad));
}
DEV float fold32(float a, float b) {
  auto r = __builtin_amdgcn_permlane32_swap(__float_as_uint(a), __float_as_uint(b), false, false);
  return __uint_as_float(r[0]) + __uint_as_float(r[1]);
}
template <int R, int N> DEV void keep(v4u (&s)[R][N]) {
  asm volatile("s_waitcnt vmcnt(0)" ::: "memory");
#pragma unroll
  for (int r = 0; r < R; ++r)
#pragma unroll
    for (int i = 0; i < N; ++i) asm volatile("" :: "v"(s[r][i]));
}
template <int SLEEP> DEV u32 gather1(rsrc_t ws, u32 voff, u32 tag) {
  v2u r;
#pragma nounroll
  for (u32 it = 0; it < SPIN_G; ++it) {
    r = __builtin_amdgcn_raw_buffer_load_b64(ws, voff, 0, 16);
    if (__all(r.y == tag)) break;
    CBAR();
    __builtin_amdgcn_s_sleep(SLEEP);
  }
  return r.x;
}
DEV void put2s(rsrc_t ws, u32 voff, u32 soff, u32 a, u32 b, u32 tag) { __builtin_amdgcn_raw_buffer_store_b128(v4u{a, tag, b, tag}, ws, voff, soff, 16); }
DEV void put2(rsrc_t ws, u32 voff, u32 a, u32 b, u32 tag) { __builtin_amdgcn_raw_buffer_store_b128(v4u{a, tag, b, tag}, ws, voff, 0, 16); }

#define TS(i) do { } while (0)
#define TT(i) do { } while (0)
#define TP(i) TS(i)

DEV u32 get_word(LAS char* s, u32 off) {
  v2u v;
#pragma nounroll
  for (u32 i = 0; i < SPIN_L; ++i) {
    v = *(volatile LAS v2u*)(s + off);
    if (__builtin_amdgcn_readfirstlane(v.x)) break;
    __builtin_amdgcn_s_sleep(1);
  }
  CBAR();
  return __builtin_amdgcn_readfirstlane(v.y);
}
DEV u32 get_tag(LAS char* s) { return get_word(s, C_TAG) + 1u; }

DEV void load64(rsrc_t w, u32 stride, u32 soff, int lane, v4u (&r)[16]) {
  const u32 voff = (u32)(lane >> 3) * 8u * stride + (u32)(lane & 7) * 16u;
#pragma unroll
  for (int j = 0; j < 16; ++j) r[j] = __builtin_amdgcn_raw_buffer_load_b128(w, voff, soff + (u32)(64 * (j >> 3) + (j & 7)) * stride, W_AUX);
  CBAR();
}
template <int N> DEV void perm8(const v4u* r, v4u* f) {
#pragma unroll
  for (int d = 0; d < 4; ++d)
#pragma unroll
    for (int i = 0; i < 4; ++i) {
      f[2 * d][i] = perm_lo(r[2 * i][d], r[2 * i + 1][d]);
      f[2 * d + 1][i] = perm_hi(r[2 * i][d], r[2 * i + 1][d]);
    }
#pragma unroll
  for (int i = 0; i < 8; ++i) asm volatile("" : "+v"(f[i]));
}
DEV void perm16(const v4u (&r)[16], v4u (&f)[16]) {
#pragma unroll
  for (int p = 0; p < 2; ++p)
#pragma unroll
    for (int d = 0; d < 4; ++d)
#pragma unroll
      for (int i = 0; i < 4; ++i) {
        f[8 * p + 2 * d][i] = perm_lo(r[8 * p + 2 * i][d], r[8 * p + 2 * i + 1][d]);
        f[8 * p + 2 * d + 1][i] = perm_hi(r[8 * p + 2 * i][d], r[8 * p + 2 * i + 1][d]);
      }
#pragma unroll
  for (int i = 0; i < 16; ++i) asm volatile("" : "+v"(f[i]));
}
DEV v4f mma64(v4u a0, v4u a1, const v4u (&f)[16], bool lowh) {
  v4f c[8];
#pragma unroll
  for (int i = 0; i < 8; ++i) c[i] = mfma(a0, f[i], v4f{0.f, 0.f, 0.f, 0.f});
#pragma unroll
  for (int i = 0; i < 8; ++i) c[i] = mfma(a1, f[8 + i], c[i]);
  v4f y;
#pragma unroll
  for (int i = 0; i < 4; ++i) y[i] = (lowh ? c[i][0] : c[4 + i][1]) + dppf<0x128>(lowh ? c[4 + i][0] : c[i][1]);
  return y;
}
DEV float quadg_sum(float v) { v += dppf<0xB1>(v); v += dppf<0x4E>(v); return sum32(sum16(v)); }

template <int C> DEV void dma_k(u32 voff, const char* sbase, u32 ldst) {
  u32 keep;
  asm volatile("s_mov_b32 %0, m0\n\ts_mov_b32 m0, %3\n\ts_nop 0\n\tglobal_load_lds_dwordx4 %1, %2 offset:%4 nt\n\ts_mov_b32 m0, %0"
               : "=&s"(keep) : "v"(voff), "s"(sbase), "s"(ldst), "i"(C * 1024) : "memory");
}
DEV void pace256(u32 due) {
#pragma nounroll
  for (u32 i = 0; i < 4096u && (int)((u32)__builtin_amdgcn_s_memrealtime() * 256u - due) < 0; ++i) __builtin_amdgcn_s_sleep(1);
  CBAR();
}
#define WAITVM(n) asm volatile("s_waitcnt vmcnt(%0)" :: "i"(n) : "memory")
#define FU(x) __float_as_uint(x)
#define UF(x) __uint_as_float(x)

__global__ void __launch_bounds__(1024, 4)
fused_decode_kernel(const char* __restrict__ px, const char* __restrict__ pk, const char* __restrict__ pv,
                    const char* __restrict__ pwq, const char* __restrict__ pwo, const char* __restrict__ pwg,
                    const char* __restrict__ pwu, const char* __restrict__ pwd, const char* __restrict__ pc,
                    const char* __restrict__ pwm, char* pout, char* pws) {
  __shared__ __attribute__((aligned(16))) char smem_[LDS_BYTES];
  LAS char* sm = (LAS char*)smem_;
  const int lane = threadIdx.x & 63;
  const int bx = blockIdx.x & 7, by = blockIdx.x >> 3;
  const int b = by >> 3, sp = by & 7;
  const bool isQ = (u32)(sp - 1) < 2u;
  const int pw = __builtin_amdgcn_readfirstlane(threadIdx.x >> 6);
  const int wave = isQ ? pw ^ 12 : (pw + ROT) & 15;
  const int gi = b * 8 + bx;
  const int hq = bx ^ XSW;
  const int g = lane >> 4, n = lane & 15;
  const int qb = n >> 2, qs = n & 3;
  const bool isWO = (u32)(sp - 3) < 2u, isFFN = ((0x21 >> sp) & (int)(gi < 28)) != 0;
  const int fi = 2 * gi + (sp == 5);
  const int nun = (UNITS >> (4 * sp)) & 15;
  TS(0);
  asm volatile("" : "+s"(px), "+s"(pk), "+s"(pv), "+s"(pwq), "+s"(pwo), "+s"(pwg), "+s"(pwu), "+s"(pwd), "+s"(pc), "+s"(pwm), "+s"(pout), "+s"(pws));
  const rsrc_t ws = mkrsrc(pws, WS_END);
  const int v = (wave - 1) & 3;
  const u64 tstart = __builtin_amdgcn_s_memrealtime();
  u32 tag0 = 0u;
  if (wave >= nun) __builtin_amdgcn_s_setprio(PRIO);
  if (wave == 15) tag0 = *(const volatile CAS u32*)(pws + WS_EPOCH);
  if (wave == 14) {
    *lp<u32>(sm, L_CTL + lane * 4) = 0u;
    asm volatile("s_waitcnt lgkmcnt(0)" ::: "memory");
  }
  auto post_tag = [&]() { if (wave == 15) { *(volatile LAS v2u*)(sm + C_TAG) = v2u{1u, tag0}; CBAR(); } };
  const bool isM = wave >= 12;
  if (isQ && wave >= 8 && (!isM || b < 3)) {
    const u32 k0 = (u32)(v * 128 + (qs >> 1) * 64 + g * 16 + (qs & 1) * 8);
    v4u own = *(const GAS v4u*)((isM ? pc : px) + qb * 1024 + k0 * 2);
    v4u wf[16];
    {
      v4u wr[16], xv[4];
      if (!isM) {
#pragma unroll
        for (int i = 0; i < 4; ++i) xv[i] = *(const GAS v4u*)(px + qb * 1024 + (qs + 4 * g) * 64 + i * 16);
        CBAR();
        __builtin_amdgcn_s_barrier();
      }
      const u32 stride = isM ? 6144u : 8192u;
      load64(mkrsrc(isM ? pwm : pwq, 512u * stride), stride, (u32)(v * 128) * stride + (u32)((isM ? gi : hq * 4 + b) * 256 + (sp - 1) * 128), lane, wr);
      if (isM) {
        __builtin_amdgcn_s_barrier();
        TS(8);
        post_tag();
#pragma unroll
        for (int i = 0; i < 4; ++i) own[i] = pk2(silu(bflo(own[i])), silu(bfhi(own[i])));
      } else {
        TS(1);
        float s = 0.f;
#pragma unroll
        for (int i = 0; i < 4; ++i)
#pragma unroll
          for (int k = 0; k < 4; ++k) s += bflo(xv[i][k]) + bfhi(xv[i][k]);
        const float mf = quadg_sum(s) * (1.f / 512.f);
        float q = 0.f;
#pragma unroll
        for (int i = 0; i < 4; ++i)
#pragma unroll
          for (int k = 0; k < 4; ++k) { float d0 = bflo(xv[i][k]) - mf, d1 = bfhi(xv[i][k]) - mf; q += d0 * d0 + d1 * d1; }
        const float var = rb(quadg_sum(q) * (1.f / 512.f));
        const float rs = __builtin_amdgcn_rcpf(rb(__builtin_amdgcn_sqrtf(rb(var + 1e-5f))));
        const float m = rb(mf);
#pragma unroll
        for (int i = 0; i < 4; ++i) own[i] = pk2(rb(bflo(own[i]) - m) * rs, rb(bfhi(own[i]) - m) * rs);
      }
      asm volatile("" : "+v"(own));
      perm16(wr, wf);
    }
    const u32 tag = get_tag(sm);
    if (isM) { TS(9); } else {
      TS(2);
      v4u mg[4];
      const u32 mo = WS_MG + (u32)(qb * 12288) + k0 * 4u;
      sleep_until(get_word(sm, C_REF) + HT0);
#pragma unroll
      for (int i = 0; i < 4; ++i) mg[i] = __builtin_amdgcn_raw_buffer_load_b128(ws, mo, (i & 1) * 2048 + (i >> 1) * 16, 16);
#pragma nounroll
      for (u32 it = 0; it < SPIN_G; ++it) {
        bool bad[4], any = false;
#pragma unroll
        for (int i = 0; i < 4; ++i) { bad[i] = (mg[i].y != tag) | (mg[i].w != tag); any = any | bad[i]; }
        if (!__any(any)) break;
        CBAR();
        if (HSLEEP) __builtin_amdgcn_s_sleep(HSLEEP);
#pragma unroll
        for (int i = 0; i < 4; ++i)
          if (bad[i]) mg[i] = __builtin_amdgcn_raw_buffer_load_b128(ws, mo, (i & 1) * 2048 + (i >> 1) * 16, 16);
      }
#pragma unroll
      for (int i = 0; i < 4; ++i) {
        const u32 s2 = (i & 1) ? mg[i & 2].z : mg[i & 2].x, c2 = (i & 1) ? mg[(i & 2) + 1].z : mg[(i & 2) + 1].x;
        own[i] = pk2(rb(bflo(own[i]) * rb(1.f + bflo(c2))) + bflo(s2), rb(bfhi(own[i]) * rb(1.f + bfhi(c2))) + bfhi(s2));
      }
      TS(3);
    }
    v4u a0, a1;
#pragma unroll
    for (int i = 0; i < 4; ++i) { a0[i] = dppu<0x44>(own[i]); a1[i] = dppu<0xEE>(own[i]); }
    const v4f r = mma64(a0, a1, wf, n < 8);
    const u32 lpart = (isM ? L_BIG : L_PART) + (u32)lane * 16u;
    *lp<v4f>(sm, lpart + (u32)(v * 1024)) = r;
    if (lds_inc_ret(sm, isM ? C_M : C_P, lane) == 3) {
      v4f p[4];
#pragma unroll
      for (int i = 0; i < 4; ++i) p[i] = *lp<v4f>(sm, lpart + (u32)(i * 1024));
      const v4f a = (p[0] + p[1]) + (p[2] + p[3]);
      put2(ws, (isM ? WS_MG + (u32)(g * 12288 + gi * 512) : WS_QG + (u32)(g * 16384 + (hq * 4 + b) * 512)) + (u32)((sp - 1) * 256 + (n & 7) * 32 + (n >> 3) * 16),
           pk2(a[0], a[1]), pk2(a[2], a[3]), tag);
    }
    if (isM) { TS(10); } else { TS(4); }
  } else if (isQ ? isM : wave >= 13) {
    __builtin_amdgcn_s_barrier();
    post_tag();
    if (by == 0 && wave == 15 && lane < 8) __builtin_amdgcn_raw_buffer_store_b64(v2u{(u32)tstart, tag0 + 1u}, ws, WS_TG + (u32)(lane * 128 + bx * 8), 0, 16);
  }

  if (wave < nun) {
    const int unit = (int)((unit_bases() >> (8 * sp)) & 255) + wave;
    const size_t rowoff = ((size_t)(gi * 4096 + unit * 64)) * 256;
    const u32 rr = ROTK ? (u32)((unit + gi * 5) & 15) << 10 : 0u;
    const char* kb = pk + rowoff - 3072;
    const rsrc_t vr = mkrsrc(pv + rowoff, 16384);
    const u32 lk = (u32)(uintptr_t)sm + (u32)wave * 16384u;
    u32 kvo[4];
#pragma unroll
    for (int c = 0; c < 4; ++c) kvo[c] = (u32)g * 256u + (u32)((n ^ (4 * g + c)) * 16) + (((u32)c * 1024u) ^ (rr & 3072u)) + (u32)(3 - c) * 1024u;
    v4u vv[16];
    const u32 pt0 = isQ ? PQ0 : PT0;
    const u32 step = (u32)((PT1 - pt0) * 8) / (u32)nun, per = step * (u32)nun;
    u32 due = ((u32)tstart + pt0) * 256u + (u32)wave * step;
    __builtin_amdgcn_s_barrier();
    TS(1);
#define KSTEP(i)                                                                         \
    { pace256(due); due += per;                                                          \
      dma_k<(i) & 3>(kvo[(i) & 3], kb + ((u32)(((i) >> 2) * 4096) ^ (rr & 12288u)), lk + ((i) >> 2) * 4096); }
    KSTEP(0) KSTEP(1) KSTEP(2) KSTEP(3) KSTEP(4) KSTEP(5) KSTEP(6) KSTEP(7)
    KSTEP(8) KSTEP(9) KSTEP(10) KSTEP(11) KSTEP(12) KSTEP(13) KSTEP(14) KSTEP(15)
    TS(11);
    const u32 ref = get_word(sm, C_REF);
    const u32 perv = ((ref + PT1) * 256u + (u32)wave * step - due) >> 4;
#pragma unroll
    for (int j = 0; j < 16; ++j) {
      pace256(due); due += perv;
      if (j == KSIG) { WAITVM(KSIG); lds_add(sm, C_K, lane, 1u); TS(10); }
      vv[j] = __builtin_amdgcn_raw_buffer_load_b128(vr, (u32)lane * 16u, (u32)(j * 1024) ^ rr, KV_NT ? 2 : 0);
      __builtin_amdgcn_sched_barrier(0);
      CBAR();
    }
    TS(2);
    const u32 tag = get_tag(sm);
    lds_wait<1>(sm, C_LS, 3);
    TS(3);
    v4u pa[2];
#pragma unroll
    for (int u = 0; u < 2; ++u) pa[u] = *lp<v4u>(sm, L_P + (u32)(wave * 512 + u * 256 + g * 64 + qb * 16));
    if (wave + nun < 9) {
      *lp<v4u>(sm, (u32)(wave + nun) * 2048u + (u32)lane * 32u) = v4u{0u, 0u, 0u, 0u};
      *lp<v4u>(sm, (u32)(wave + nun) * 2048u + (u32)lane * 32u + 16) = v4u{0u, 0u, 0u, 0u};
    }
    u32 la = (u32)wave * 2048u + (u32)lane * 32u, lf = F_O + (u32)wave * 4u, one = 1u;
    u32 lr = (u32)wave * 512u + (u32)lane * 8u, lq = F_O + (u32)(n < nun ? n : 0) * 4u;
    u32 po = WS_OP + (u32)(((((bx * 4 + wave) * 4 + b) * 2) * 8 + sp) * 512) + (u32)(lane >> 5) * 4096u + (u32)(lane & 31) * 16u;
    PIN(la); PIN(lf); PIN(one); PIN(lr); PIN(lq); PIN(po);
    v4f oa[2] = {{0.f, 0.f, 0.f, 0.f}, {0.f, 0.f, 0.f, 0.f}};
#pragma unroll
    for (int u = 0; u < 2; ++u) {
      v4u bv[8], pm[4];
#pragma unroll
      for (int t = 0; t < 4; ++t) { pm[t] = qs == t ? pa[u] : v4u{0u, 0u, 0u, 0u}; PIN(pm[t]); }
#pragma unroll
      for (int i = 0; i < 4; ++i) {
#pragma unroll
        for (int d = 0; d < 4; ++d) {
          bv[2 * d][i] = perm_lo(vv[8 * u + 2 * i][d], vv[8 * u + 2 * i + 1][d]);
          bv[2 * d + 1][i] = perm_hi(vv[8 * u + 2 * i][d], vv[8 * u + 2 * i + 1][d]);
        }
        if (i < 3) __builtin_amdgcn_sched_barrier(0);
      }
#pragma unroll
      for (int t = 0; t < 4; ++t) {
        oa[0] = mfma(pm[t], bv[t], oa[0]);
        oa[1] = mfma(pm[t], bv[4 + t], oa[1]);
      }
      __builtin_amdgcn_sched_barrier(0);
    }
    *lp<v4f>(sm, la) = oa[0];
    *lp<v4f>(sm, la + 16) = oa[1];
    CBAR();
    *(volatile LAS u32*)(sm + lf) = one;
    TS(5);
    if (wave < 4) {
      v4u pq = {0u, tag, 0u, tag};
      PIN(pq);
      {
        u32 left = SPIN_L, f, thr;
#pragma nounroll
        do { f = *(volatile LAS u32*)(sm + lq); --left; thr = left ? 1u : 0u; } while (__builtin_amdgcn_ballot_w64(f < thr) != 0ull);
        CBAR();
      }
      TS(6);
      v2f op[9];
#pragma unroll
      for (int w = 0; w < 9; ++w) op[w] = *lp<v2f>(sm, lr + w * 2048);
      const v2f o = (((op[0] + op[1]) + (op[2] + op[3])) + ((op[4] + op[5]) + (op[6] + op[7]))) + op[8];
      pq.x = FU(o[0]); pq.z = FU(o[1]);
      __builtin_amdgcn_raw_buffer_store_b128(pq, ws, po, 0, 16);
      lds_add(sm, C_PUB, lane, 1u);
      TS(7);
    }
    if (isWO) {
      if (wave < 8) {
        __builtin_amdgcn_s_setprio(PRIO);
        v4u og[4];
        float wgt[4];
        u32 wok = 0, left = SPIN_G, bad[4], any;
        const u32 oo = WS_OP + (u32)(((hq * 4 + b) * 8 + wave) * 4096 + lane * 16), wo = L_WS + (u32)((wave >> 1) * 32 + (lane >> 5) * 4);
        u32 lo = L_ACT + (u32)((wave >> 1) * 1088 + (wave & 1) * 128 + (lane & 31) * 4 + (lane >> 5) * 2), fa = F_A + (u32)wave * 4u;
        PIN(lo); PIN(fa);
#pragma unroll
        for (int i = 0; i < 4; ++i) { og[i] = v4u{0u, 0u, 0u, 0u}; bad[i] = 1u; }
        sleep_until(ref + OT0 - 12u);
#pragma nounroll
        do {
          slot_wait(ref + OT0);
#pragma unroll
          for (int i = 0; i < 4; ++i)
            if (bad[i]) og[i] = __builtin_amdgcn_raw_buffer_load_b128(ws, oo, i * 1024, 16);
          CBAR();
          __builtin_amdgcn_s_sleep(WSL);
          wok = *(volatile LAS u32*)(sm + C_W);
#pragma unroll
          for (int i = 0; i < 4; ++i) wgt[i] = *(volatile LAS float*)(sm + wo + i * 8);
          --left;
          const u32 lim = left ? ~0u : 0u;
          wok = __builtin_amdgcn_readfirstlane(wok);
          asm volatile("" : "+s"(wok));
          __builtin_amdgcn_sched_barrier(0);
#pragma unroll
          for (int i = 0; i < 4; ++i) bad[i] = tagbad(og[i], tag);
          any = ((bad[0] | bad[1]) | (bad[2] | bad[3])) & lim;
        } while (anynz(any));
        TT(8);
        if (__builtin_expect(!wok, 0)) {
          lds_wait<0>(sm, C_W, 1);
#pragma unroll
          for (int i = 0; i < 4; ++i) wgt[i] = *(volatile LAS float*)(sm + wo + i * 8);
        }
        const float o0 = (wgt[0] * UF(og[0].x) + wgt[1] * UF(og[1].x)) + (wgt[2] * UF(og[2].x) + wgt[3] * UF(og[3].x));
        const float o1 = (wgt[0] * UF(og[0].z) + wgt[1] * UF(og[1].z)) + (wgt[2] * UF(og[2].z) + wgt[3] * UF(og[3].z));
        const float of = fold32(o0, o1);
        *lp<unsigned short>(sm, lo) = (unsigned short)pk2(of, of);
        CBAR();
        *(volatile LAS u32*)(sm + fa) = one;
        TT(9);
      }
    } else if (isFFN && (wave & 12) == 4) {
      __builtin_amdgcn_s_setprio(PRIO);
      const int v = wave - 4;
      v4u wf[8];
      v2f sb[4], cb1[4];
      {
        lds_wait<2>(sm, C_PUB, 4);
        v4u wr[8];
        const rsrc_t wd = mkrsrc(pwd, 1792 * 512 * 2);
#pragma unroll
        for (int i = 0; i < 8; ++i) wr[i] = __builtin_amdgcn_raw_buffer_load_b128(wd, (u32)(g * 8192 + n * 16), (u32)(fi * 32768 + v * 256 + i * 1024), W_AUX);
        CBAR();
        lds_wait<2>(sm, F_S + (u32)(v >> 1) * 4u, 1);
        const u32 co = (u32)(((lane >> 5) * 64 + (lane & 3) * 16 + (bx & 1) * 8 + ((lane >> 3) & 3) * 2 + ((lane >> 2) & 1)) * 2);
#pragma unroll
        for (int i = 0; i < 8; ++i) {
          const u32 c = (co ^ (u32)((i & 1) * 16)) + (u32)((i >> 1) * 256);
          sb[i >> 1][i & 1] = bflo(*lp<unsigned short>(sm, L_ACT + (u32)(v * 1088) + c));
          cb1[i >> 1][i & 1] = bflo(*lp<unsigned short>(sm, L_PART + (u32)(v * 1024) + c));
        }
        perm8<0>(wr, wf);
      }
      u32 o = WS_FP + (u32)(((g * 4 + v) * 2 + (n >> 3)) * 4) * FPS + (u32)(fi * 128 + (n & 7) * 16);
      u32 lh = L_ACT + (u32)(v * 1088 + lane * 16), fa = F_A + (u32)v * 4u, ff = F_F + (u32)qs * 4u, lpp = L_PART + (u32)(qb * 256 + g * 64 + qs * 16);
      const u32 xo = WS_XG + (u32)(bx * 16384 + v * 4096 + lane * 16);
      v4u fq[4];
#pragma unroll
      for (int i = 0; i < 4; ++i) { fq[i] = v4u{0u, tag, 0u, tag}; PIN(sb[i]); PIN(cb1[i]); PIN(fq[i]); }
      PIN(o); PIN(lh); PIN(fa); PIN(ff); PIN(lpp);
      TS(4);
      v2f dd[4];
      float piv;
      v2f s1, s2;
      {
        u32 left = SPIN_G, bad;
        sleep_until(ref + OT0 + PH_X - 12u);
#pragma nounroll
        do {
          slot_wait(ref + OT0 + PH_X);
          v4u xg[4];
#pragma unroll
          for (int i = 0; i < 4; ++i) xg[i] = __builtin_amdgcn_raw_buffer_load_b128(ws, xo, i * 1024, 16);
          --left;
          const u32 lim = left ? ~0u : 0u;
          __builtin_amdgcn_sched_barrier(0);
          piv = UF(__builtin_amdgcn_readfirstlane(xg[0].x));
#pragma unroll
          for (int i = 0; i < 4; ++i) {
            bad = i ? bad | tagbad(xg[i], tag) : tagbad(xg[i], tag);
            dd[i] = v2f{UF(xg[i].x) - piv, UF(xg[i].z) - piv};
            s1 = i ? s1 + dd[i] : dd[i];
            s2 = i ? dd[i] * dd[i] + s2 : dd[i] * dd[i];
            PIN(bad); PIN(dd[i]); PIN(s1); PIN(s2);
            __builtin_amdgcn_sched_barrier(0);
          }
          bad &= lim;
        } while (anynz(bad));
      }
      TT(8);
      const float st = rowsum(sum16(fold32(s1[0] + s1[1], s2[0] + s2[1])));
      const float md = UF(__builtin_amdgcn_readlane(FU(st), 0)) * (1.f / 512.f);
      const float var = rb(fmaxf(UF(__builtin_amdgcn_readlane(FU(st), 32)) * (1.f / 512.f) - md * md, 0.f));
      const float sh = piv - rb(piv + md);
      const float rs = __builtin_amdgcn_rcpf(rb(__builtin_amdgcn_sqrtf(rb(var + 1e-5f))));
      v4u hv;
#pragma unroll
      for (int i = 0; i < 4; ++i) {
        const v2f t = ((dd[i] + sh) * cb1[i]) * rs + sb[i];
        hv[i] = pk2(t[0], t[1]);
      }
      *lp<v4u>(sm, lh) = hv;
      CBAR();
      *(volatile LAS u32*)(sm + fa) = one;
      TT(9);
      v4f p[4];
      {
        u32 left = SPIN_L, f, thr;
#pragma nounroll
        do {
          f = *(volatile LAS u32*)(sm + ff);
#pragma unroll
          for (int i = 0; i < 4; ++i) p[i] = *(volatile LAS v4f*)(sm + lpp + (u32)(i * 1024));
          --left; thr = left ? 1u : 0u;
        } while (__builtin_amdgcn_ballot_w64(f < thr) != 0ull);
      }
      TT(10);
      const v4f gu = (p[0] + p[1]) + (p[2] + p[3]);
      const u32 ac = pk2(silu(gu[0]) * gu[2], silu(gu[1]) * gu[3]);
      const v4u a = {dppu<0x00>(ac), dppu<0x55>(ac), dppu<0xAA>(ac), dppu<0xFF>(ac)};
      v4f c[8];
#pragma unroll
      for (int i = 0; i < 8; ++i) c[i] = mfma(a, wf[i], v4f{0.f, 0.f, 0.f, 0.f});
      __builtin_amdgcn_sched_barrier(0);
#pragma unroll
      for (int i = 0; i < 4; ++i) {
        fq[i].x = FU(c[2 * i][0]); fq[i].z = FU(c[2 * i + 1][0]);
        __builtin_amdgcn_raw_buffer_store_b128(fq[i], ws, o, i * FPS, 16);
      }
      TP(11);
    }
  } else if (wave >= 13) {
    const int j = wave - 13;
    const u32 tag = get_tag(sm);
    TS(1);
    lds_wait<4>(sm, C_K, nun);
    TS(2);
    const u32 krow = (u32)j * 49152u + (u32)(4 * qs + qb) * 256u;
    u32 ko[4];
#pragma unroll
    for (int ks = 0; ks < 4; ++ks) ko[ks] = krow + (u32)(((4 * ks + g) ^ n) * 16);
    v4u kf[12];
#pragma unroll
    for (int i = 0; i < 12; ++i) kf[i] = *lp<v4u>(sm, ko[i / 3] + (i % 3) * 16384);
    lds_wait<2>(sm, C_QRDY, 1);
    TS(3);
    v4u qf[4];
#pragma unroll
    for (int ks = 0; ks < 4; ++ks) qf[ks] = *lp<v4u>(sm, L_Q + (u32)qb * 320u + ks * 64 + g * 16);
    v4f sc[3];
#pragma unroll
    for (int w = 0; w < 3; ++w) sc[w] = v4f{0.f, 0.f, 0.f, 0.f};
#pragma unroll
    for (int t = 0; t < 4; ++t) {
#pragma unroll
      for (int ks = 0; ks < 4; ++ks) {
        const v4u qm = qs == t ? qf[ks] : v4u{0u, 0u, 0u, 0u};
#pragma unroll
        for (int w = 0; w < 3; ++w) {
          sc[w] = mfma(kf[3 * ks + w], qm, sc[w]);
          if (t < 3) kf[3 * ks + w] = *lp<v4u>(sm, ko[ks] + w * 16384 + (t + 1) * 4096);
        }
        __builtin_amdgcn_sched_barrier(0);
      }
    }
    TS(4);
    float mx = -__builtin_inff();
#pragma unroll
    for (int w = 0; w < 3; ++w) {
      const bool valid = 3 * j + w < nun;
#pragma unroll
      for (int r = 0; r < 4; r += 2) {
        const u32 s2 = pk2(sc[w][r], sc[w][r + 1]);
        u32 t2 = pk2(bflo(s2) * 0.08838834764831845f, bfhi(s2) * 0.08838834764831845f);
        PIN(t2);
        t2 = valid ? t2 : 0xff80ff80u;
        sc[w][r] = bflo(t2); sc[w][r + 1] = bfhi(t2);
        mx = fmaxf(mx, fmaxf(sc[w][r], sc[w][r + 1]));
      }
    }
    mx = fmaxf(mx, dppf<0xB1>(mx)); mx = fmaxf(mx, dppf<0x4E>(mx));
    mx = max32(max16(mx));
    *lp<float>(sm, L_MX + (u32)qb * 16u + j * 4) = mx;
    lds_add(sm, C_MX, lane, 1u);
    lds_wait<0>(sm, C_MX, 3);
    const v4f m0 = *lp<v4f>(sm, L_MX + (u32)qb * 16u);
    mx = fmaxf(fmaxf(m0[0], m0[1]), m0[2]);
    TS(5);
    float ls = 0.f;
#pragma unroll
    for (int w = 0; w < 3; ++w) {
#pragma unroll
      for (int r = 0; r < 4; ++r) sc[w][r] = __expf(sc[w][r] - mx);
      ls += (sc[w][0] + sc[w][1]) + (sc[w][2] + sc[w][3]);
      *lp<v2u>(sm, L_P + (u32)(j * 1536 + w * 512 + (qs >> 1) * 256 + g * 64 + qb * 16 + (qs & 1) * 8)) = v2u{pk2(sc[w][0], sc[w][1]), pk2(sc[w][2], sc[w][3])};
    }
    ls = quadg_sum(ls);
    *lp<float>(sm, L_LS + (u32)qb * 16u + j * 4) = ls;
    if (lds_inc_ret(sm, C_LS, lane) == 2u) {
      const v4f l0 = *lp<v4f>(sm, L_LS + (u32)qb * 16u);
      ls = (l0[0] + l0[1]) + l0[2];
      if ((lane & 0x33) == 0) put2(ws, WS_ML + (u32)((((bx * 4 + qb) * 4 + b) * 8 + sp) * 16), FU(mx), FU(ls), tag);
      TS(6);
      if (isWO) {
        v4u ml[1];
        sleep_until(get_word(sm, C_REF) + MT1);
        gather2<1, 0, 8>(ws, WS_ML + (u32)(((hq * 4 + b) * 32 + (lane & 31)) * 16), tag, ml);
        const float ms = UF(ml[0].x);
        float M = fmaxf(ms, dppf<0xB1>(ms)); M = fmaxf(M, dppf<0x4E>(M)); M = fmaxf(M, dppf<0x141>(M));
        const float e = __expf(ms - M);
        float L = e * UF(ml[0].z);
        L += dppf<0xB1>(L); L += dppf<0x4E>(L); L += dppf<0x141>(L);
        *lp<float>(sm, L_WS + (u32)(lane & 31) * 4u) = e * __builtin_amdgcn_rcpf(L);
        lds_set(sm, C_W);
        TS(7);
      }
    }
  } else {
    const bool main4 = wave >= 9;
    if (!isQ || wave < 8) {
      u32 pf = 0u;
      if (PFQ && main4 && (!isWO || b < PFQ - 1)) {
        const u32 m = sp == 0 ? 0u : (u32)(sp - 4);
        const char* a = !isWO ? pwq + (u32)((m * 128u + (u32)((v >> 1) * 64 + lane)) * 8192u + (u32)((hq * 4 + b) * 256 + (v & 1) * 128))
                              : pwm + (u32)((u32)(v * 128 + 64 + lane) * 6144u + (u32)(gi * 256 + (sp - 3) * 128));
        pf = PF_AUX ? __builtin_nontemporal_load((const GAS u32*)a) : *(const GAS u32*)a;
        CBAR();
      }
      __builtin_amdgcn_s_barrier();
      asm volatile("" :: "v"(pf));
      TS(1);
    }
    const u32 tag = get_tag(sm);
    if (wave == (isQ ? 7 : 12)) {
      sleep_until((u32)tstart + RT0);
      int d = (int)(gather1<8>(ws, WS_TG + (u32)(bx * 128 + (lane & 7) * 8), tag) - (u32)tstart);
      d = min(d, (int)dppu<0xB1>((u32)d)); d = min(d, (int)dppu<0x4E>((u32)d)); d = min(d, (int)dppu<0x141>((u32)d));
      *(volatile LAS v2u*)(sm + C_REF) = v2u{1u, (u32)tstart + (u32)max(min(d, 100), -200)};
    }
    if (wave == 12) {
      sleep_until(get_word(sm, C_REF) + QT0);
      v2u qg[2];
      v4u qsets[QR][2];
      const u32 qo = WS_QG + (u32)(b * 16384 + bx * 2048 + lane * 16);
      gatherp<2, 1024, QR, QGAP>(ws, qo, tag, qg, qsets);
      *lp<v2u>(sm, L_Q + (u32)(lane >> 5) * 320u + (u32)(lane & 31) * 8u) = qg[0];
      *lp<v2u>(sm, L_Q + 640 + (u32)(lane >> 5) * 320u + (u32)(lane & 31) * 8u) = qg[1];
      lds_set(sm, C_QRDY);
      TS(11);
      keep(qsets);
    }
    if (main4 && (isWO | isFFN)) {
      const int head = hq * 4 + b, hf = sp - 3;
      if (isFFN && wave < 11) {
        const u32 mo = WS_MG + (u32)(v * 24576 + 6144 + lane * 16);
        sleep_until((u32)tstart + SBT0);
        v4u mg[8];
        u32 left = SPIN_G, bad;
#pragma nounroll
        do {
          __builtin_amdgcn_s_sleep(8);
#pragma unroll
          for (int i = 0; i < 8; ++i) mg[i] = __builtin_amdgcn_raw_buffer_load_b128(ws, mo, (i >> 2) * 12288 + (i & 3) * 1024, 16);
          --left;
          bad = 0u;
#pragma unroll
          for (int i = 0; i < 8; ++i) bad |= tagbad(mg[i], tag);
          bad &= left ? ~0u : 0u;
        } while (anynz(bad));
#pragma unroll
        for (int i = 0; i < 8; ++i) {
          if (i & 2) *lp<v2u>(sm, L_PART + (u32)(v * 2048 + lane * 8 + (i >> 2) * 1024 + (i & 1) * 512)) =
                         v2u{pk2(1.f + bflo(mg[i].x), 1.f + bfhi(mg[i].x)), pk2(1.f + bflo(mg[i].z), 1.f + bfhi(mg[i].z))};
          else *lp<v2u>(sm, L_ACT + (u32)(v * 2176 + lane * 8 + (i >> 2) * 1088 + (i & 1) * 512)) = v2u{mg[i].x, mg[i].z};
        }
        lds_set(sm, F_S + (u32)v * 4u);
        TS(4);
      }
      if (!isWO) { sleep_until(get_word(sm, C_REF) + GT0); lds_wait<4>(sm, C_PUB, 4); }
      const u32 wstep = isWO ? (PW1 - PW0) * 32u : 0u;
      u32 due = ((u32)tstart + (isWO ? PW0 : 0u)) * 256u + (u32)v * wstep + (u32)(b * 2 + hf) * (wstep >> 3);
      TS(5);
      v4u wf[16];
      {
        v4u wr[16];
        const char* wp = isWO ? pwo + (u32)((head * 128 + (lane >> 3) * 8) * 1024 + hf * 512 + v * 128 + (lane & 7) * 16)
                              : ((lane & 4) ? pwu : pwg) + (u32)(((v >> 1) * 64 + (g & 1) * 32 + (n >> 3) * 16 + (v & 1) * 4 + (g >> 1)) * 3584 + fi * 64 + qs * 16);
        const u32 s0 = isWO ? 1024u : 8u * 3584u, s1 = isWO ? 2048u : 128u * 3584u, s3 = isWO ? 65536u : 2u * 3584u, par = isWO ? 0u : (u32)(bx & 1), flip = sp == 5;
#pragma unroll
        for (int j = 0; j < 16; ++j) {
          const GAS v4u* a = (const GAS v4u*)(wp + (((u32)(j & 1) ^ par) * s0 + (u32)((j >> 1) & 3) * s1 + ((u32)(j >> 3) ^ flip) * s3));
          if ((j & 7) == 0) { pace256(due); due += 4u * wstep; }
          wr[j] = W_AUX ? __builtin_nontemporal_load(a) : *a;
          __builtin_amdgcn_sched_barrier(0);
        }
        CBAR();
        perm16(wr, wf);
      }
      u32 ao = L_ACT + (u32)(qb * 1088 + g * 32 + (qs & 1) * 16 + (isWO ? 0 : v * 256)), ao1 = ao + (sp == 5 ? 0u : 128u), fa = F_A + (u32)(lane & (isWO ? 7 : 3)) * 4u;
      u32 o = isWO ? WS_WP + (u32)((((g * 2 + hf) * 4 + v) * 2 + (n >> 3)) * 2) * WPS + (u32)(head * 128 + (n & 7) * 16) : L_PART + (u32)(v * 1024 + g * 256 + qs * 64 + (n >> 3) * 32 + (qb & 1) * 8);
      u32 ff = F_F + (u32)v * 4u, one = 1u;
      v4u q0 = {0u, tag, 0u, tag}, q1 = q0;
      ao += sp == 5 ? 128u : 0u;
      PIN(ao); PIN(ao1); PIN(fa); PIN(o); PIN(ff); PIN(one); PIN(q0); PIN(q1);
      TS(6);
      v4u a0, a1;
      {
        u32 left = SPIN_L, f, thr;
#pragma nounroll
        do {
          f = *(volatile LAS u32*)(sm + fa);
          a0 = *(volatile LAS v4u*)(sm + ao); a1 = *(volatile LAS v4u*)(sm + ao1);
          --left; thr = left ? 1u : 0u;
        } while (__builtin_amdgcn_ballot_w64(f < thr) != 0ull);
      }
      TT(9);
      const v4f r = mma64(a0, a1, wf, n < 8);
      if (isWO) {
        q0.x = FU(r[0]); q0.z = FU(r[1]); q1.x = FU(r[2]); q1.z = FU(r[3]);
        __builtin_amdgcn_raw_buffer_store_b128(q0, ws, o, 0, 16);
        __builtin_amdgcn_raw_buffer_store_b128(q1, ws, o, WPS, 16);
      } else {
        *lp<v2f>(sm, o) = v2f{r[0], r[1]};
        *lp<v2f>(sm, o + 16) = v2f{r[2], r[3]};
        CBAR();
        *(volatile LAS u32*)(sm + ff) = one;
      }
      TP(10);
    } else if (isQ ? wave == 9 : (sp == 6 && main4 && wave < 11)) {
      const int task = gi * 4 + (isQ ? sp - 1 : wave - 7), tb = task >> 5;
      const u32 pr = (u32)(((task >> 2) & 7) * 32 + (lane & 7) * 4 + (task & 3));
      const bool hi = lane >= 32;
      const u32 xp = *(const GAS u32*)(px + tb * 1024 + pr * 4);
      sleep_until((u32)tstart + SBT0);
      const u32 ga2 = gather1<8>(ws, WS_MG + (u32)(tb * 12288 + 4096) + pr * 8u, tag);
      const u32 gb2 = gather1<8>(ws, WS_MG + (u32)(tb * 12288 + 10240) + pr * 8u, tag);
      float xv = hi ? bfhi(xp) : bflo(xp), ga = hi ? bfhi(ga2) : bflo(ga2), gb = hi ? bfhi(gb2) : bflo(gb2);
      u32 oo = (u32)(tb * 1024) + pr * 4u + (u32)(lane >> 5) * 2u;
      const rsrc_t outr = mkrsrc(pout, 4096);
      PIN(gb); PIN(oo);
      u32 xo = WS_XG + (u32)((((lane >> 3) & 3) * 2 + (lane & 1)) * 16384 + task * 128 + (lane >> 5) * 64 + ((lane >> 1) & 3) * 16);
      v4u xq = {0u, tag, 0u, tag};
      PIN(xv); PIN(ga); PIN(xo); PIN(xq);
      const u32 ref = get_word(sm, C_REF) + OT0;
      TS(5);
      float a0, a1;
      gathersum<4>(ws, WS_WP + (u32)task * WPS + (u32)lane * 16u, tag, ref + PH_B, a0, a1);
      const u32 tf = (now32() - ref - PH_B) / SLOT * SLOT + ref + PH_F;
      TT(6);
      a0 = fold32(a0, a1);
      a0 += dppf<0x128>(a0);
      const float x1 = rb(xv + ga * sum16(a0));
      xq.x = FU(x1); xq.z = dppu<0xB1>(FU(x1));
      __builtin_amdgcn_raw_buffer_store_b128(xq, ws, xo, 0, 16);
      TP(7);
      gathersum<7>(ws, WS_FP + (u32)task * FPS + (u32)lane * 16u, tag, tf, a0, a1);
      TT(8);
      a0 = fold32(a0, a1);
      a0 += dppf<0x128>(a0);
      const float y = x1 + gb * sum16(a0);
      __builtin_amdgcn_raw_buffer_store_b16((unsigned short)pk2(y, y), outr, oo, 0, 0);
      if (task == 0 && lane == 0) __builtin_amdgcn_raw_buffer_store_b32(tag, ws, WS_EPOCH, 0, 16);
      TS(9);
    } else if (PFT && wave == 11 && gi < 28 && !isWO) {
      sleep_until(get_word(sm, C_REF) + FT0);
      lds_wait<4>(sm, C_PUB, 4);
      const char* a = isQ ? pwd + (u32)(gi * 65536 + (sp - 1) * 32768 + lane * 128) : (sp == 6 ? pwg : pwu) + (u32)((256 + lane) * 3584 + gi * 128);
      const u32 st = isQ ? 8192u : 64u * 3584u;
      u32 pf[4];
#pragma unroll
      for (int i = 0; i < 4; ++i) pf[i] = *(const GAS u32*)(a + (u32)i * st);
#pragma unroll
      for (int i = 0; i < 4; ++i) asm volatile("" :: "v"(pf[i]));
    }
  }
}

extern "C" void kernel_launch(void* const* d_in, const int* in_sizes, int n_in,
                              void* d_out, int out_size, void* d_ws, size_t ws_size,
                              hipStream_t stream) {
  fused_decode_kernel<<<256, 1024, 0, stream>>>(
      (const char*)d_in[0], (const char*)d_in[1], (const char*)d_in[2], (const char*)d_in[3], (const char*)d_in[4],
      (const char*)d_in[6], (const char*)d_in[7], (const char*)d_in[8], (const char*)d_in[9], (const char*)d_in[10],
      (char*)d_out, (char*)d_ws);
}
```

```cpp
#include <hip/hip_runtime.h>
#include <stdint.h>

typedef unsigned int u32;
typedef unsigned long long u64;
typedef u32 v4u __attribute__((ext_vector_type(4)));
typedef u32 v2u __attribute__((ext_vector_type(2)));
typedef float v4f __attribute__((ext_vector_type(4)));
typedef float v2f __attribute__((ext_vector_type(2)));
typedef __bf16 v8bf __attribute__((ext_vector_type(8)));
typedef __bf16 v2bf __attribute__((ext_vector_type(2)));
typedef __amdgpu_buffer_rsrc_t rsrc_t;
#define LAS __attribute__((address_space(3)))
#define CAS __attribute__((address_space(4)))
#define GAS __attribute__((address_space(1)))
#define DEV __device__ __forceinline__
#define CBAR() asm volatile("" ::: "memory")

#define UNITS 0x99899668u
#define KSIG 4
#define SBT0 700
#define PT0 10
#define PT1 1120
#define PQ0 400
#define WD 0
#define GD 270
#define WNT 1
#define DT0 (PT1 + DD)
#define WA0 (PT1 + WD)
#define WAL 50
#define WPM 4
#define FST 5
#define KPRO 1
#define ROTK 1
#define RT0 200
#define OT0 (PT1 + OD)
#define GT0 (PT1 + GD)
#define FT0 (PT1 + FD)
#define FD 85
#define FS 40
#define FS2 FS
#define DD GD
#define FPM 1
#define QSL 16
#define TLBW 400
#define WARM 150
#define PAGES 1
#define XSW 4
#define MT1 (PT1 + 40)
#define OD 206
#define OAUX 16
#define OSAUX 16
#define GAUX 16
#define GRACE 20
#define PRE 12u
#define OPRE 30u
#define CPF 5
#define RET_X 16
#define RET_F 40
#define KV_NT 1
#define W_AUX 0
#define PRIO 3
#define QT0 740
#define QR 1
#define QGAP 16
#define HSLEEP 16
#define ROT 15
#define PFQ 2
#define HT0 470
#define WSL 4
#define SLOT 64
#define PH_B 128
#define PH_X (PH_B + 62)
#define PH_F (PH_X + 148)

constexpr int unit_count(int s) { return (UNITS >> (4 * s)) & 15; }
constexpr int unit_base(int s) { return s == 0 ? 0 : unit_base(s - 1) + unit_count(s - 1); }
constexpr u64 unit_bases() { u64 r = 0; for (int s = 0; s < 8; ++s) r |= (u64)unit_base(s) << (8 * s); return r; }
constexpr u64 steps(int s0) { u64 r = 0; for (int s = s0; s < s0 + 4; ++s) r |= (u64)((PT1 - (s == 1 || s == 2 ? PQ0 : PT0)) * 8 / unit_count(s)) << (16 * (s - s0)); return r; }
static_assert(unit_base(8) == 64 && unit_count(1) <= 7 && unit_count(2) <= 7 && unit_count(1) >= 5 && unit_count(2) >= 5, "unit table");
static_assert(unit_count(0) >= 8 && unit_count(3) >= 5 && unit_count(4) >= 5 && unit_count(5) >= 8 && unit_count(6) >= 5 && unit_count(7) >= 5, "unit table");

constexpr u32 WS_EPOCH = 0;
constexpr u32 WS_TG = 1024;
constexpr u32 WS_MG = 4096;
constexpr u32 WS_QG = WS_MG + 8 * 49152;
constexpr u32 WS_XG = WS_QG + 65536;
constexpr u32 WS_ML = WS_XG + 131072;
constexpr u32 WS_OP = WS_ML + 16384;
constexpr u32 WPS = 33 * 128, FPS = 57 * 128;
constexpr u32 WS_WP = WS_OP + 524288;
constexpr u32 WS_FP = WS_WP + 128 * WPS;
constexpr u32 WS_END = WS_FP + 128 * FPS;
constexpr u32 OOB = 0x7ff00000u;

constexpr u32 L_BIG = 8 * 16384;
constexpr u32 L_Q = 147456;
constexpr u32 L_P = L_Q + 1280;
constexpr u32 L_ACT = L_P + 4608;
constexpr u32 L_PART = L_ACT + 4352;
constexpr u32 L_MX = L_PART + 4096;
constexpr u32 L_LS = L_MX + 64;
constexpr u32 L_WS = L_LS + 64;
constexpr u32 L_CTL = L_WS + 128;
constexpr u32 C_TAG = L_CTL, C_GO = L_CTL + 8, C_ISS = L_CTL + 12, C_QRDY = L_CTL + 16, C_K = L_CTL + 20, C_MX = L_CTL + 24,
              C_LS = L_CTL + 28, C_O = L_CTL + 32, C_A = L_CTL + 36, C_P = L_CTL + 40, C_W = L_CTL + 44, C_F = L_CTL + 48,
              C_KI = L_CTL + 52, C_WL = L_CTL + 56, C_M = L_CTL + 60, C_PUB = L_CTL + 64, C_W1 = L_CTL + 68, C_REF = L_CTL + 72, C_T0 = L_CTL + 80;
constexpr u32 F_S = L_CTL + 96, F_A = L_CTL + 128, F_F = L_CTL + 160, F_O = L_CTL + 192;
constexpr u32 L_SCR = L_CTL + 256;
constexpr u32 LDS_BYTES = L_SCR + 256;

constexpr u32 SPIN_G = 1u << 18;
constexpr u32 SPIN_L = 1u << 22;

DEV float bflo(u32 v) { return __uint_as_float(v << 16); }
DEV float bfhi(u32 v) { return __uint_as_float(v & 0xffff0000u); }
DEV u32 pk2(float a, float b) { v2f f = {a, b}; v2bf h = __builtin_convertvector(f, v2bf); return __builtin_bit_cast(u32, h); }
DEV float rb(float f) { return bflo(pk2(f, f)); }
DEV v4f mfma(v4u a, v4u b, v4f c) {
  return __builtin_amdgcn_mfma_f32_16x16x32_bf16(__builtin_bit_cast(v8bf, a), __builtin_bit_cast(v8bf, b), c, 0, 0, 0);
}
DEV u32 perm_lo(u32 even, u32 odd) { return __builtin_amdgcn_perm(odd, even, 0x05040100u); }
DEV u32 perm_hi(u32 even, u32 odd) { return __builtin_amdgcn_perm(odd, even, 0x07060302u); }
template <int CTRL> DEV u32 dppu(u32 v) { return __builtin_amdgcn_update_dpp(0u, v, CTRL, 0xf, 0xf, true); }
template <int CTRL> DEV float dppf(float v) { return __uint_as_float(dppu<CTRL>(__float_as_uint(v))); }
DEV float sum16(float v) { u32 x = __float_as_uint(v); auto r = __builtin_amdgcn_permlane16_swap(x, x, false, false); return __uint_as_float(r[0]) + __uint_as_float(r[1]); }
DEV float sum32(float v) { u32 x = __float_as_uint(v); auto r = __builtin_amdgcn_permlane32_swap(x, x, false, false); return __uint_as_float(r[0]) + __uint_as_float(r[1]); }
DEV float max16(float v) { u32 x = __float_as_uint(v); auto r = __builtin_amdgcn_permlane16_swap(x, x, false, false); return fmaxf(__uint_as_float(r[0]), __uint_as_float(r[1])); }
DEV float max32(float v) { u32 x = __float_as_uint(v); auto r = __builtin_amdgcn_permlane32_swap(x, x, false, false); return fmaxf(__uint_as_float(r[0]), __uint_as_float(r[1])); }
DEV float rowsum(float v) {
  v += dppf<0xB1>(v);
  v += dppf<0x4E>(v);
  v += dppf<0x141>(v);
  v += dppf<0x140>(v);
  return v;
}
DEV float silu(float v) { return v * __builtin_amdgcn_rcpf(1.f + __expf(-v)); }

DEV rsrc_t mkrsrc(const void* p, u32 bytes) { return __builtin_amdgcn_make_buffer_rsrc((void*)p, (short)0, (int)bytes, 0x00020000); }

template <typename T> DEV LAS T* lp(LAS char* s, u32 off) { return (LAS T*)(s + off); }

template <int SLEEP> DEV void lds_wait(LAS char* s, u32 off, u32 target) {
#pragma nounroll
  for (u32 i = 0; i < SPIN_L; ++i) {
    u32 v = __builtin_amdgcn_readfirstlane(*(volatile LAS u32*)(s + off));
    if (v >= target) break;
    if (SLEEP) __builtin_amdgcn_s_sleep(SLEEP);
  }
  CBAR();
}
DEV void lds_add(LAS char* s, u32 off, int lane, u32 v) {
  CBAR();
  if (lane == 0) __hip_atomic_fetch_add((LAS u32*)(s + off), v, __ATOMIC_RELAXED, __HIP_MEMORY_SCOPE_WORKGROUP);
  CBAR();
}
DEV u32 lds_inc_ret(LAS char* s, u32 off, int lane) {
  CBAR();
  u32 r = 0;
  if (lane == 0) r = __hip_atomic_fetch_add((LAS u32*)(s + off), 1u, __ATOMIC_RELAXED, __HIP_MEMORY_SCOPE_WORKGROUP);
  r = __builtin_amdgcn_readfirstlane(r);
  CBAR();
  return r;
}
DEV void lds_set(LAS char* s, u32 off) { CBAR(); *(volatile LAS u32*)(s + off) = 1u; CBAR(); }
template <int N, int STRIDE, int SLEEP> DEV void gather2(rsrc_t ws, u32 voff, u32 tag, v4u (&r)[N], u32 vlast) {
#pragma unroll
  for (int i = 0; i < N; ++i) r[i] = __builtin_amdgcn_raw_buffer_load_b128(ws, i == N - 1 ? vlast : voff, i * STRIDE, 16);
#pragma nounroll
  for (u32 it = 0; it < SPIN_G; ++it) {
    bool bad[N], any = false;
#pragma unroll
    for (int i = 0; i < N; ++i) { bad[i] = (r[i].y != tag) | (r[i].w != tag); any = any | bad[i]; }
    if (!__any(any)) break;
    CBAR();
    if (SLEEP) __builtin_amdgcn_s_sleep(SLEEP);
#pragma unroll
    for (int i = 0; i < N; ++i)
      if (bad[i]) r[i] = __builtin_amdgcn_raw_buffer_load_b128(ws, i == N - 1 ? vlast : voff, i * STRIDE, 16);
  }
}
template <int N, int STRIDE, int SLEEP> DEV void gather2(rsrc_t ws, u32 voff, u32 tag, v4u (&r)[N]) { gather2<N, STRIDE, SLEEP>(ws, voff, tag, r, voff); }
template <int OFF> DEV void ldg(v4u& r, rsrc_t ws, u32 vo) {
  asm volatile("buffer_load_dwordx4 %0, %1, %2, 0 offen offset:%3 sc1" : "+v"(r) : "v"(vo), "s"(ws), "i"(OFF) : "memory");
}
template <int N, int STRIDE> DEV void ldset(v4u (&s)[N], rsrc_t ws, const u32 (&vo)[N]) {
  ldg<0>(s[0], ws, vo[0]);
  if constexpr (N > 1) ldg<STRIDE>(s[1], ws, vo[1]);
  if constexpr (N > 2) ldg<2 * STRIDE>(s[2], ws, vo[2]);
  if constexpr (N > 3) ldg<3 * STRIDE>(s[3], ws, vo[3]);
}
template <int N> DEV u32 chkset(v4u (&s)[N], u32 tag, v2u (&res)[N], u32 (&vo)[N]) {
  u32 miss = 0;
#pragma unroll
  for (int i = 0; i < N; ++i) {
    asm volatile("" : "+v"(s[i]));
    const bool good = (vo[i] != OOB) & (s[i].y == tag) & (s[i].w == tag);
    res[i].x = good ? s[i].x : res[i].x; res[i].y = good ? s[i].z : res[i].y;
    vo[i] = good ? OOB : vo[i];
    miss += (u32)__builtin_popcountll(__builtin_amdgcn_ballot_w64(vo[i] != OOB));
    asm volatile("" : "+v"(res[i]), "+v"(vo[i]));
  }
  return miss;
}
template <int N, int STRIDE, int R, int GAP> DEV void gatherp(rsrc_t ws, u32 voff, u32 tag, v2u (&res)[N], v4u (&s)[R][N]) {
  u32 vo[N];
#pragma unroll
  for (int i = 0; i < N; ++i) { vo[i] = voff; res[i] = v2u{0u, 0u}; }
#pragma unroll
  for (int r = 0; r < R; ++r) {
#pragma unroll
    for (int i = 0; i < N; ++i) asm volatile("" : "=v"(s[r][i]));
    ldset<N, STRIDE>(s[r], ws, vo);
    if (r < R - 1) __builtin_amdgcn_s_sleep(GAP);
  }
#pragma nounroll
  for (u32 it = 0; it < SPIN_G; ++it) {
#pragma unroll
    for (int r = 0; r < R; ++r) {
      asm volatile("s_waitcnt vmcnt(%0)" :: "i"((R - 1) * N) : "memory");
      if (chkset<N>(s[r], tag, res, vo) == 0) return;
      if (QSL) __builtin_amdgcn_s_sleep(QSL);
      ldset<N, STRIDE>(s[r], ws, vo);
    }
  }
}
DEV u32 now32() { return (u32)__builtin_amdgcn_s_memrealtime(); }
DEV void sleep_until(u32 t) {
#pragma nounroll
  for (u32 i = 0; i < 4096u && (int)(now32() - t) < 0; ++i) __builtin_amdgcn_s_sleep(2);
  CBAR();
}
DEV u32 slot_wait(u32 first) {
  const u32 t = now32() - GRACE, d = first - t;
  const u32 end = t + ((int)d < 0 ? d % (u32)SLOT : d);
#pragma nounroll
  for (u32 i = 0; i < 4096u && (int)(now32() - end) < 0; ++i) { }
  CBAR();
  return end;
}
#define PAGE() asm volatile("s_branch 9f\n\t.p2align 12\n9:")
#define PAD() asm volatile("7:\n\ts_cbranch_execz 7f")
#define PADL(i) asm volatile(".Lwarm" #i ":\n7:\n\ts_cbranch_execz 7f")
#define PADEND() asm volatile("s_branch 8f\n7:\n\ts_setpc_b64 vcc\n8:")
DEV void warm_code(int j, u32 t0) {
  sleep_until(t0 + WARM);
  u64 keep;
  asm volatile("s_mov_b64 %0, exec\n\ts_getpc_b64 vcc\n.Lpc%=:\n\ts_add_u32 vcc_lo, vcc_lo, .Lret%=-.Lpc%=\n\ts_addc_u32 vcc_hi, vcc_hi, 0\n\ts_mov_b64 exec, 0\n\t"
               "s_cmp_eq_u32 %1, 0\n\ts_cbranch_scc1 .Lwarm0\n\ts_cmp_eq_u32 %1, 1\n\ts_cbranch_scc1 .Lwarm1\n\ts_branch .Lwarm2\n"
               ".Lret%=:\n\ts_mov_b64 exec, %0" : "=&s"(keep) : "s"(j) : "vcc", "scc");
}
DEV u32 tagbad(const v4u& r, u32 tag) { return __builtin_amdgcn_bitop3_b32(r.y, r.w, tag, 0x7e); }
DEV bool anynz(u32 v) { return __builtin_amdgcn_ballot_w64(v != 0u) != 0ull; }
#define PIN(x) asm volatile("" : "+v"(x))
template <int N> DEV u32 gathersum(rsrc_t ws, u32 voff, u32 tag, u32 first, u32 later, float& a0, float& a1) {
  sleep_until(first - PRE);
  u32 left = SPIN_G, bad, at;
#pragma nounroll
  do {
    at = slot_wait(first);
    first = later;
    v4u r[N];
#pragma unroll
    for (int i = 0; i < N; ++i) r[i] = __builtin_amdgcn_raw_buffer_load_b128(ws, voff, i * 1024, GAUX);
    --left;
    const u32 lim = left ? ~0u : 0u;
    PAD();
    __builtin_amdgcn_sched_barrier(0);
    bad = tagbad(r[0], tag); a0 = __uint_as_float(r[0].x); a1 = __uint_as_float(r[0].z);
#pragma unroll
    for (int i = 1; i < N; ++i) {
      bad |= tagbad(r[i], tag); a0 += __uint_as_float(r[i].x); a1 += __uint_as_float(r[i].z);
      PIN(bad); PIN(a0); PIN(a1);
      __builtin_amdgcn_sched_barrier(0);
    }
    bad &= lim;
  } while (anynz(bad));
  return at;
}
DEV float fold32(float a, float b) {
  auto r = __builtin_amdgcn_permlane32_swap(__float_as_uint(a), __float_as_uint(b), false, false);
  return __uint_as_float(r[0]) + __uint_as_float(r[1]);
}
DEV float fold16(float a, float b) {
  auto r = __builtin_amdgcn_permlane16_swap(__float_as_uint(a), __float_as_uint(b), false, false);
  return __uint_as_float(r[0]) + __uint_as_float(r[1]);
}
template <int R, int N> DEV void keep(v4u (&s)[R][N]) {
  asm volatile("s_waitcnt vmcnt(0)" ::: "memory");
#pragma unroll
  for (int r = 0; r < R; ++r)
#pragma unroll
    for (int i = 0; i < N; ++i) asm volatile("" :: "v"(s[r][i]));
}
template <int SLEEP> DEV u32 gather1(rsrc_t ws, u32 voff, u32 tag) {
  v2u r;
#pragma nounroll
  for (u32 it = 0; it < SPIN_G; ++it) {
    r = __builtin_amdgcn_raw_buffer_load_b64(ws, voff, 0, 16);
    if (__all(r.y == tag)) break;
    CBAR();
    __builtin_amdgcn_s_sleep(SLEEP);
  }
  return r.x;
}
DEV void put2s(rsrc_t ws, u32 voff, u32 soff, u32 a, u32 b, u32 tag) { __builtin_amdgcn_raw_buffer_store_b128(v4u{a, tag, b, tag}, ws, voff, soff, 16); }
DEV void put2(rsrc_t ws, u32 voff, u32 a, u32 b, u32 tag) { __builtin_amdgcn_raw_buffer_store_b128(v4u{a, tag, b, tag}, ws, voff, 0, 16); }

template <int SLEEP = 1> DEV u32 get_word(LAS char* s, u32 off) {
  v2u v;
#pragma nounroll
  for (u32 i = 0; i < SPIN_L; ++i) {
    v = *(volatile LAS v2u*)(s + off);
    if (__builtin_amdgcn_readfirstlane(v.x)) break;
    __builtin_amdgcn_s_sleep(SLEEP);
  }
  CBAR();
  return __builtin_amdgcn_readfirstlane(v.y);
}
DEV u32 get_tag(LAS char* s) { return get_word(s, C_TAG) + 1u; }

DEV void load64(rsrc_t w, u32 stride, u32 soff, int lane, v4u (&r)[16]) {
  const u32 voff = (u32)(lane >> 3) * 8u * stride + (u32)(lane & 7) * 16u;
#pragma unroll
  for (int j = 0; j < 16; ++j) r[j] = __builtin_amdgcn_raw_buffer_load_b128(w, voff, soff + (u32)(64 * (j >> 3) + (j & 7)) * stride, W_AUX);
  CBAR();
}
template <int N> DEV void perm8(const v4u* r, v4u* f) {
#pragma unroll
  for (int d = 0; d < 4; ++d)
#pragma unroll
    for (int i = 0; i < 4; ++i) {
      f[2 * d][i] = perm_lo(r[2 * i][d], r[2 * i + 1][d]);
      f[2 * d + 1][i] = perm_hi(r[2 * i][d], r[2 * i + 1][d]);
    }
#pragma unroll
  for (int i = 0; i < 8; ++i) asm volatile("" : "+v"(f[i]));
}
DEV void perm16(const v4u (&r)[16], v4u (&f)[16]) {
#pragma unroll
  for (int p = 0; p < 2; ++p)
#pragma unroll
    for (int d = 0; d < 4; ++d)
#pragma unroll
      for (int i = 0; i < 4; ++i) {
        f[8 * p + 2 * d][i] = perm_lo(r[8 * p + 2 * i][d], r[8 * p + 2 * i + 1][d]);
        f[8 * p + 2 * d + 1][i] = perm_hi(r[8 * p + 2 * i][d], r[8 * p + 2 * i + 1][d]);
      }
#pragma unroll
  for (int i = 0; i < 16; ++i) asm volatile("" : "+v"(f[i]));
}
DEV v4f mma64(v4u a0, v4u a1, const v4u (&f)[16], bool lowh) {
  v4f c[8];
#pragma unroll
  for (int i = 0; i < 8; ++i) c[i] = mfma(a0, f[i], v4f{0.f, 0.f, 0.f, 0.f});
#pragma unroll
  for (int i = 0; i < 8; ++i) c[i] = mfma(a1, f[8 + i], c[i]);
  v4f y;
#pragma unroll
  for (int i = 0; i < 4; ++i) y[i] = (lowh ? c[i][0] : c[4 + i][1]) + dppf<0x128>(lowh ? c[4 + i][0] : c[i][1]);
  return y;
}
DEV float quadg_sum(float v) { v += dppf<0xB1>(v); v += dppf<0x4E>(v); return sum32(sum16(v)); }

template <int C> DEV void dma_k(u32 voff, const char* sbase, u32 ldst) {
  u32 keep;
  asm volatile("s_mov_b32 %0, m0\n\ts_mov_b32 m0, %3\n\ts_nop 0\n\tglobal_load_lds_dwordx4 %1, %2 offset:%4 nt\n\ts_mov_b32 m0, %0"
               : "=&s"(keep) : "v"(voff), "s"(sbase), "s"(ldst), "i"(C * 1024) : "memory");
}
DEV void dma_sc1(u32 voff, const char* sbase, u32 ldst) {
  u32 keep;
  asm volatile("s_mov_b32 %0, m0\n\ts_mov_b32 m0, %3\n\ts_nop 0\n\tglobal_load_lds_dwordx4 %1, %2 sc1\n\ts_mov_b32 m0, %0"
               : "=&s"(keep) : "v"(voff), "s"(sbase), "s"(ldst) : "memory");
}
DEV void pace256(u32 due) {
#pragma nounroll
  for (u32 i = 0; i < 4096u && (int)((u32)__builtin_amdgcn_s_memrealtime() * 256u - due) < 0; ++i) __builtin_amdgcn_s_sleep(1);
  CBAR();
}
#define WAITVM(n) asm volatile("s_waitcnt vmcnt(%0)" :: "i"(n) : "memory")
#define FU(x) __float_as_uint(x)
#define UF(x) __uint_as_float(x)

__global__ void __launch_bounds__(1024, 4)
fused_decode_kernel(const char* __restrict__ px, const char* __restrict__ pk, const char* __restrict__ pv,
                    const char* __restrict__ pwq, const char* __restrict__ pwo, const char* __restrict__ pwg,
                    const char* __restrict__ pwu, const char* __restrict__ pwd, const char* __restrict__ pc,
                    const char* __restrict__ pwm, char* pout, char* pws) {
  __shared__ __attribute__((aligned(16))) char smem_[LDS_BYTES];
  LAS char* sm = (LAS char*)smem_;
  const int pw = __builtin_amdgcn_readfirstlane(threadIdx.x >> 6);
  const char* code; u32 clast;
  {
    u32 co = threadIdx.x * 128u, keep;
    asm volatile("s_getpc_b64 %0\n.Lcode%=:\n\ts_mov_b32 %1, fused_code_end-.Lcode%=-4\n\ts_cmp_ge_u32 %4, %5\n\ts_cbranch_scc1 .Lskip%=\n\t"
                 "s_mov_b32 %2, m0\n\ts_mov_b32 m0, %6\n\tv_min_u32 %3, %1, %3\n\ts_nop 0\n\tglobal_load_lds_dword %3, %0\n\ts_mov_b32 m0, %2\n.Lskip%=:"
                 : "=&s"(code), "=&s"(clast), "=&s"(keep), "+v"(co) : "s"(pw), "i"(CPF), "s"((u32)(uintptr_t)sm + L_SCR) : "scc", "memory");
  }
  const int lane = threadIdx.x & 63;
  const int bx = blockIdx.x & 7, by = blockIdx.x >> 3;
  const int b = by >> 3, sp = by & 7;
  const bool isQ = (u32)(sp - 1) < 2u;
  const int wave = isQ ? pw ^ 12 : (pw + ROT) & 15;
  const int gi = b * 8 + bx;
  const int hq = bx ^ XSW;
  const int g = lane >> 4, n = lane & 15;
  const int qb = n >> 2, qs = n & 3;
  const bool isWO = (0xd8 >> sp) & 1, isFFN = ((0x21 >> sp) & (int)(gi < 28)) != 0;
  const int fi = 2 * gi + (sp == 5);
  const int nun = (UNITS >> (4 * sp)) & 15;
  asm volatile("" : "+s"(px), "+s"(pk), "+s"(pv), "+s"(pwq), "+s"(pwo), "+s"(pwg), "+s"(pwu), "+s"(pwd), "+s"(pc), "+s"(pwm), "+s"(pout), "+s"(pws));
  const rsrc_t ws = mkrsrc(pws, WS_END);
  const int v = (wave - 1) & 3;
  const u32 mgr = WS_MG + (u32)bx * 49152u;
  const u64 tstart = __builtin_amdgcn_s_memrealtime();
  u32 tag0 = 0u;
  asm volatile("" :: "s"(tstart));
  const int unit = (int)((unit_bases() >> (8 * sp)) & 255) + wave;
  const size_t rowoff = ((size_t)(gi * 4096 + unit * 64)) * 256;
  const u32 rr = ROTK ? (u32)((unit + gi * 5) & 15) << 10 : 0u;
  const char* kb = pk + rowoff - 3072;
  const u32 lk = (u32)(uintptr_t)sm + (u32)wave * 16384u;
  auto kvoff = [&](u32 t, int c) { return (t & 48u) * 16u + (((t & 15u) ^ (((t >> 2) & 12u) + (u32)c)) * 16u) + (((u32)c * 1024u) ^ (rr & 3072u)) + (u32)(3 - c) * 1024u; };
  if (!isQ) {
    if (wave < nun) {
      if (KPRO > 0) dma_k<0>(kvoff(threadIdx.x, 0), kb + (rr & 12288u), lk);
      if (KPRO > 1) dma_k<1>(kvoff(threadIdx.x, 1), kb + (rr & 12288u), lk);
    } else if ((u32)(wave - 9) < 4u) {
      const bool m34 = (u32)(sp - 3) < 2u;
      if (!m34 || b < PFQ - 1) {
        const u32 m = sp == 0 ? 0u : (u32)(sp - 4);
        const char* a = !m34 ? pwq + (u32)((m * 128u + (u32)((v >> 1) * 64)) * 8192u + (u32)((hq * 4 + b) * 256 + (v & 1) * 128))
                              : pwm + (u32)((u32)(v * 128 + 64) * 6144u + (u32)(gi * 256 + (sp - 3) * 128));
        u32 keep;
        asm volatile("s_mov_b32 %0, m0\n\ts_mov_b32 m0, %3\n\ts_nop 0\n\tglobal_load_lds_dword %1, %2\n\ts_mov_b32 m0, %0"
                     : "=&s"(keep) : "v"((u32)lane * (m34 ? 6144u : 8192u)), "s"(a), "s"((u32)(uintptr_t)sm + L_SCR) : "memory");
      }
    }
  }
  if (wave >= nun) __builtin_amdgcn_s_setprio(PRIO);
  if (wave == 15) tag0 = *(const volatile CAS u32*)(pws + WS_EPOCH);
  if (wave == 14) {
    *lp<u32>(sm, L_CTL + lane * 4) = 0u;
    asm volatile("s_waitcnt lgkmcnt(0)" ::: "memory");
  }
  auto post_tag = [&]() { if (wave == 15) { *(volatile LAS v2u*)(sm + C_TAG) = v2u{1u, tag0}; CBAR(); } };
  const bool isM = wave >= 12;
  const bool early = isQ && wave >= 8 && (!isM || b < 3);
  if (!early) {
    __builtin_amdgcn_s_barrier();
    post_tag();
    if (by == 0 && wave == 15 && lane < 8) __builtin_amdgcn_raw_buffer_store_b64(v2u{(u32)tstart, tag0 + 1u}, ws, WS_TG + (u32)(lane * 128 + bx * 8), 0, 16);
  }
  const u32 step = (u32)((sp < 4 ? steps(0) : steps(4)) >> (16 * (sp & 3))) & 0xffffu;
  u32 due = ((u32)tstart + (isQ ? PQ0 : PT0)) * 256u + (u32)wave * step;
  if (wave < nun) {
    const u32 per = isQ ? (PT1 - PQ0) * 8u : (PT1 - PT0) * 8u;
    u32 kvo[4], tid = threadIdx.x;
    PIN(tid);
#pragma unroll
    for (int c = 0; c < 4; ++c) kvo[c] = kvoff(tid, c);
    u32 skip = isQ ? 0u : 1u;
#pragma nounroll
    for (u32 i = 0; i < 16384u; i += 4096u) {
      const char* a = kb + (i ^ (rr & 12288u));
      if (KPRO < 1 || !skip) { pace256(due); dma_k<0>(kvo[0], a, lk + i); }
      due += per;
      if (KPRO < 2 || !skip) { pace256(due); dma_k<1>(kvo[1], a, lk + i); }
      due += per;
      skip = 0u;
      pace256(due); dma_k<2>(kvo[2], a, lk + i); due += per;
      pace256(due); dma_k<3>(kvo[3], a, lk + i); due += per;
    }
  }
  if (early) {
    const u32 k0 = (u32)(v * 128 + (qs >> 1) * 64 + g * 16 + (qs & 1) * 8);
    v4u own = *(const GAS v4u*)((isM ? pc : px) + qb * 1024 + k0 * 2);
    v4u wf[16];
    {
      v4u wr[16], xv[4];
      if (!isM) {
#pragma unroll
        for (int i = 0; i < 4; ++i) xv[i] = *(const GAS v4u*)(px + qb * 1024 + (qs + 4 * g) * 64 + i * 16);
        CBAR();
        __builtin_amdgcn_s_barrier();
      }
      const u32 stride = isM ? 6144u : 8192u;
      load64(mkrsrc(isM ? pwm : pwq, 512u * stride), stride, (u32)(v * 128) * stride + (u32)((isM ? gi : hq * 4 + b) * 256 + (sp - 1) * 128), lane, wr);
      if (isM) {
        __builtin_amdgcn_s_barrier();
        post_tag();
#pragma unroll
        for (int i = 0; i < 4; ++i) own[i] = pk2(silu(bflo(own[i])), silu(bfhi(own[i])));
      } else {
        float s = 0.f;
#pragma unroll
        for (int i = 0; i < 4; ++i)
#pragma unroll
          for (int k = 0; k < 4; ++k) s += bflo(xv[i][k]) + bfhi(xv[i][k]);
        const float mf = quadg_sum(s) * (1.f / 512.f);
        float q = 0.f;
#pragma unroll
        for (int i = 0; i < 4; ++i)
#pragma unroll
          for (int k = 0; k < 4; ++k) { float d0 = bflo(xv[i][k]) - mf, d1 = bfhi(xv[i][k]) - mf; q += d0 * d0 + d1 * d1; }
        const float var = rb(quadg_sum(q) * (1.f / 512.f));
        const float rs = __builtin_amdgcn_rcpf(rb(__builtin_amdgcn_sqrtf(rb(var + 1e-5f))));
        const float m = rb(mf);
#pragma unroll
        for (int i = 0; i < 4; ++i) own[i] = pk2(rb(bflo(own[i]) - m) * rs, rb(bfhi(own[i]) - m) * rs);
      }
      asm volatile("" : "+v"(own));
      perm16(wr, wf);
    }
    const u32 tag = get_tag(sm);
    if (!isM) {
      v4u mg[4];
      const u32 mo = mgr + (u32)(qb * 12288) + k0 * 4u;
      sleep_until(get_word(sm, C_REF) + HT0);
#pragma unroll
      for (int i = 0; i < 4; ++i) mg[i] = __builtin_amdgcn_raw_buffer_load_b128(ws, mo, (i & 1) * 2048 + (i >> 1) * 16, 16);
#pragma nounroll
      for (u32 it = 0; it < SPIN_G; ++it) {
        bool bad[4], any = false;
#pragma unroll
        for (int i = 0; i < 4; ++i) { bad[i] = (mg[i].y != tag) | (mg[i].w != tag); any = any | bad[i]; }
        if (!__any(any)) break;
        CBAR();
        if (HSLEEP) __builtin_amdgcn_s_sleep(HSLEEP);
#pragma unroll
        for (int i = 0; i < 4; ++i)
          if (bad[i]) mg[i] = __builtin_amdgcn_raw_buffer_load_b128(ws, mo, (i & 1) * 2048 + (i >> 1) * 16, 16);
      }
#pragma unroll
      for (int i = 0; i < 4; ++i) {
        const u32 s2 = (i & 1) ? mg[i & 2].z : mg[i & 2].x, c2 = (i & 1) ? mg[(i & 2) + 1].z : mg[(i & 2) + 1].x;
        own[i] = pk2(rb(bflo(own[i]) * rb(1.f + bflo(c2))) + bflo(s2), rb(bfhi(own[i]) * rb(1.f + bfhi(c2))) + bfhi(s2));
      }
    }
    v4u a0, a1;
#pragma unroll
    for (int i = 0; i < 4; ++i) { a0[i] = dppu<0x44>(own[i]); a1[i] = dppu<0xEE>(own[i]); }
    const v4f r = mma64(a0, a1, wf, n < 8);
    const u32 lpart = (isM ? L_BIG : L_PART) + (u32)lane * 16u;
    *lp<v4f>(sm, lpart + (u32)(v * 1024)) = r;
    if (lds_inc_ret(sm, isM ? C_M : C_P, lane) == 3) {
      v4f p[4];
#pragma unroll
      for (int i = 0; i < 4; ++i) p[i] = *lp<v4f>(sm, lpart + (u32)(i * 1024));
      const v4f a = (p[0] + p[1]) + (p[2] + p[3]);
      const u32 po = (isM ? WS_MG + (u32)(g * 12288 + gi * 512) : WS_QG + (u32)(g * 16384 + (hq * 4 + b) * 512)) + (u32)((sp - 1) * 256 + (n & 7) * 32 + (n >> 3) * 16);
#pragma nounroll
      for (u32 r = 0; r < (isM ? 8u * 49152u : 1u); r += 49152u) put2s(ws, po, r, pk2(a[0], a[1]), pk2(a[2], a[3]), tag);
    }
  }

  if (wave < nun) {
    const rsrc_t vr = mkrsrc(pv + rowoff, 16384);
    v4u vv[16];
    const u32 pt1 = PT1;
    const u32 ref = get_word(sm, C_REF);
    const u32 perv = ((ref + pt1) * 256u + (u32)wave * step - due) >> 4;
#pragma unroll
    for (int j = 0; j < 16; ++j) {
      pace256(due); due += perv;
      if (j == KSIG) { WAITVM(KSIG); lds_add(sm, C_K, lane, 1u); }
      vv[j] = __builtin_amdgcn_raw_buffer_load_b128(vr, (u32)lane * 16u, (u32)(j * 1024) ^ rr, KV_NT ? 2 : 0);
      __builtin_amdgcn_sched_barrier(0);
      CBAR();
    }
    lds_add(sm, C_ISS, lane, 1u);
    PAGE(); PADL(2);
    const u32 tag = get_tag(sm);
    lds_wait<1>(sm, C_LS, 3);
    v4u pa[2];
#pragma unroll
    for (int u = 0; u < 2; ++u) pa[u] = *lp<v4u>(sm, L_P + (u32)(wave * 512 + u * 256 + g * 64 + qb * 16));
    if (wave + nun < 9) {
      *lp<v4u>(sm, (u32)(wave + nun) * 2048u + (u32)lane * 32u) = v4u{0u, 0u, 0u, 0u};
      *lp<v4u>(sm, (u32)(wave + nun) * 2048u + (u32)lane * 32u + 16) = v4u{0u, 0u, 0u, 0u};
    }
    u32 la = (u32)wave * 2048u + (u32)lane * 32u, lf = F_O + (u32)wave * 4u, one = 1u;
    u32 lr = (u32)wave * 512u + (u32)(lane & 31) * 16u, lq = F_O + (u32)(n < nun ? n : 0) * 4u;
    u32 po = lane < 32 ? WS_OP + (u32)(((((bx * 4 + wave) * 4 + b) * 2 + g) * 8 + sp) * 256 + n * 16) : OOB;
    PIN(la); PIN(lf); PIN(one); PIN(lr); PIN(lq); PIN(po);
    v4f oa[2] = {{0.f, 0.f, 0.f, 0.f}, {0.f, 0.f, 0.f, 0.f}};
#pragma unroll
    for (int u = 0; u < 2; ++u) {
      v4u bv[8], pm[4];
      PAD();
#pragma unroll
      for (int t = 0; t < 4; ++t) { pm[t] = qs == t ? pa[u] : v4u{0u, 0u, 0u, 0u}; PIN(pm[t]); }
#pragma unroll
      for (int i = 0; i < 4; ++i) {
#pragma unroll
        for (int d = 0; d < 4; ++d) {
          bv[2 * d][i] = perm_lo(vv[8 * u + 2 * i][d], vv[8 * u + 2 * i + 1][d]);
          bv[2 * d + 1][i] = perm_hi(vv[8 * u + 2 * i][d], vv[8 * u + 2 * i + 1][d]);
        }
        if (i < 3) __builtin_amdgcn_sched_barrier(0);
      }
#pragma unroll
      for (int t = 0; t < 4; ++t) {
        oa[0] = mfma(pm[t], bv[t], oa[0]);
        oa[1] = mfma(pm[t], bv[4 + t], oa[1]);
      }
      __builtin_amdgcn_sched_barrier(0);
    }
    PAD();
    *lp<v4f>(sm, la) = oa[0];
    *lp<v4f>(sm, la + 16) = oa[1];
    CBAR();
    *(volatile LAS u32*)(sm + lf) = one;
    if (wave < 4) {
      v4u pq = {0u, tag, 0u, tag};
      PIN(pq);
      {
        u32 left = SPIN_L, f, thr;
#pragma nounroll
        do { f = *(volatile LAS u32*)(sm + lq); --left; thr = left ? 1u : 0u; } while (__builtin_amdgcn_ballot_w64(f < thr) != 0ull);
        CBAR();
      }
      if (lane < 32) {
        v4f op[9];
#pragma unroll
        for (int w = 0; w < 9; ++w) op[w] = *lp<v4f>(sm, lr + w * 2048);
        const v4f o = (((op[0] + op[1]) + (op[2] + op[3])) + ((op[4] + op[5]) + (op[6] + op[7]))) + op[8];
        pq.x = pk2(o[0], o[1]); pq.z = pk2(o[2], o[3]);
        __builtin_amdgcn_raw_buffer_store_b128(pq, ws, po, 0, OSAUX);
      }
      lds_add(sm, C_PUB, lane, 1u);
    }
    if (isFFN && (wave & 12) == 4) {
      __builtin_amdgcn_s_setprio(PRIO);
      const int v = wave - 4;
      PAGE(); PAD();
      v4u wf[8];
      v2f sb[4], cb1[4];
      {
        sleep_until(ref + DT0);
        lds_wait<2>(sm, C_PUB, 4);
        v4u wr[8];
        const rsrc_t wd = mkrsrc(pwd, 1792 * 512 * 2);
#pragma unroll
        for (int i = 0; i < 8; ++i) wr[i] = __builtin_amdgcn_raw_buffer_load_b128(wd, (u32)(g * 8192 + n * 16), (u32)(fi * 32768 + v * 256 + i * 1024), 0);
        CBAR();
        lds_wait<2>(sm, F_S + (u32)(v >> 1) * 4u, 1);
        PAD();
        const u32 co = (u32)(((lane >> 5) * 64 + (lane & 3) * 16 + (bx & 1) * 8 + ((lane >> 3) & 3) * 2 + ((lane >> 2) & 1)) * 2);
#pragma unroll
        for (int i = 0; i < 8; ++i) {
          const u32 c = (co ^ (u32)((i & 1) * 16)) + (u32)((i >> 1) * 256);
          sb[i >> 1][i & 1] = bflo(*lp<unsigned short>(sm, L_ACT + (u32)(v * 1088) + c));
          cb1[i >> 1][i & 1] = bflo(*lp<unsigned short>(sm, L_PART + (u32)(v * 1024) + c));
        }
        PAD();
        perm8<0>(wr, wf);
      }
      u32 o = WS_FP + (u32)(((g * 4 + v) * 2 + (n >> 3)) * 4) * FPS + (u32)(fi * 128 + (n & 7) * 16);
      u32 lh = L_ACT + (u32)(v * 1088 + lane * 16), fa = F_A + (u32)v * 4u, ff = F_F + (u32)qs * 4u, lpp = L_PART + (u32)(qb * 256 + g * 64 + qs * 16);
      const u32 xo = WS_XG + (u32)(bx * 16384 + v * 4096 + lane * 16);
      v4u fq[4];
#pragma unroll
      for (int i = 0; i < 4; ++i) { fq[i] = v4u{0u, tag, 0u, tag}; PIN(sb[i]); PIN(cb1[i]); PIN(fq[i]); }
      PIN(o); PIN(lh); PIN(fa); PIN(ff); PIN(lpp);
      v2f dd[4];
      float piv;
      v2f s1, s2;
      {
        PAD();
        const u32 t0 = ref + OT0 + PH_X;
        u32 left = SPIN_G, bad, first = t0;
        sleep_until(first - PRE);
#pragma nounroll
        do {
          slot_wait(first);
          first = t0 + RET_X;
          v4u xg[4];
#pragma unroll
          for (int i = 0; i < 4; ++i) xg[i] = __builtin_amdgcn_raw_buffer_load_b128(ws, xo, i * 1024, GAUX);
          --left;
          const u32 lim = left ? ~0u : 0u;
          PAD();
          __builtin_amdgcn_sched_barrier(0);
          piv = UF(__builtin_amdgcn_readfirstlane(xg[0].x));
#pragma unroll
          for (int i = 0; i < 4; ++i) {
            bad = i ? bad | tagbad(xg[i], tag) : tagbad(xg[i], tag);
            dd[i] = v2f{UF(xg[i].x) - piv, UF(xg[i].z) - piv};
            s1 = i ? s1 + dd[i] : dd[i];
            s2 = i ? dd[i] * dd[i] + s2 : dd[i] * dd[i];
            PIN(bad); PIN(dd[i]); PIN(s1); PIN(s2);
            __builtin_amdgcn_sched_barrier(0);
          }
          bad &= lim;
        } while (anynz(bad));
      }
      const float st = rowsum(sum16(fold32(s1[0] + s1[1], s2[0] + s2[1])));
      const float md = UF(__builtin_amdgcn_readlane(FU(st), 0)) * (1.f / 512.f);
      const float var = rb(fmaxf(UF(__builtin_amdgcn_readlane(FU(st), 32)) * (1.f / 512.f) - md * md, 0.f));
      const float sh = piv - rb(piv + md);
      const float rs = __builtin_amdgcn_rcpf(rb(__builtin_amdgcn_sqrtf(rb(var + 1e-5f))));
      v4u hv;
#pragma unroll
      for (int i = 0; i < 4; ++i) {
        const v2f t = ((dd[i] + sh) * cb1[i]) * rs + sb[i];
        hv[i] = pk2(t[0], t[1]);
      }
      *lp<v4u>(sm, lh) = hv;
      CBAR();
      *(volatile LAS u32*)(sm + fa) = one;
      PAD();
      v4f p[4];
      {
        u32 left = SPIN_L, f, thr;
#pragma nounroll
        do {
          f = *(volatile LAS u32*)(sm + ff);
#pragma unroll
          for (int i = 0; i < 4; ++i) p[i] = *(volatile LAS v4f*)(sm + lpp + (u32)(i * 1024));
          --left; thr = left ? 1u : 0u;
        } while (__builtin_amdgcn_ballot_w64(f < thr) != 0ull);
      }
      const v4f gu = (p[0] + p[1]) + (p[2] + p[3]);
      const u32 ac = pk2(silu(gu[0]) * gu[2], silu(gu[1]) * gu[3]);
      const v4u a = {dppu<0x00>(ac), dppu<0x55>(ac), dppu<0xAA>(ac), dppu<0xFF>(ac)};
      v4f c[8];
#pragma unroll
      for (int i = 0; i < 8; ++i) c[i] = mfma(a, wf[i], v4f{0.f, 0.f, 0.f, 0.f});
      __builtin_amdgcn_sched_barrier(0);
#pragma unroll
      for (int i = 0; i < 4; ++i) {
        fq[i].x = FU(c[2 * i][0]); fq[i].z = FU(c[2 * i + 1][0]);
        __builtin_amdgcn_raw_buffer_store_b128(fq[i], ws, o, i * FPS, 16);
      }
    }
  } else if (wave >= 13) {
    const int j = wave - 13;
    const u32 tag = get_tag(sm);
    warm_code(j, (u32)tstart);
    if (TLBW && j == 1 && gi < 28) {
      sleep_until((u32)tstart + TLBW);
      u32 pf[3];
#pragma unroll
      for (int i = 0; i < 3; ++i) {
        const int c = sp * 3 + i;
        const char* a = c < 16 ? (c < 8 ? pwg : pwu) + (u32)(((c & 7) * 64 + (lane & 15) * 4 + b) * 3584 + gi * 128) : pwd + (u32)(gi * 65536 + (c & 7) * 8192 + (lane & 1) * 4096);
        pf[i] = *(const GAS u32*)a;
      }
#pragma unroll
      for (int i = 0; i < 3; ++i) asm volatile("" :: "v"(pf[i]));
    }
    if (TLBW && j == 2) {
      sleep_until((u32)tstart + TLBW);
      const u32 pf = *(const GAS u32*)(pwo + (u32)((hq * 4 + b) * 131072 + (lane & 31) * 4096));
      asm volatile("" :: "v"(pf));
    }
    PAGE(); PADL(1);
    lds_wait<4>(sm, C_K, nun);
    const u32 krow = (u32)j * 49152u + (u32)(4 * qs + qb) * 256u;
    u32 ko[4];
#pragma unroll
    for (int ks = 0; ks < 4; ++ks) ko[ks] = krow + (u32)(((4 * ks + g) ^ n) * 16);
    v4u kf[12];
#pragma unroll
    for (int i = 0; i < 12; ++i) kf[i] = *lp<v4u>(sm, ko[i / 3] + (i % 3) * 16384);
    lds_wait<2>(sm, C_QRDY, 1);
    v4u qf[4];
#pragma unroll
    for (int ks = 0; ks < 4; ++ks) qf[ks] = *lp<v4u>(sm, L_Q + (u32)qb * 320u + ks * 64 + g * 16);
    v4f sc[3];
#pragma unroll
    for (int w = 0; w < 3; ++w) sc[w] = v4f{0.f, 0.f, 0.f, 0.f};
#pragma unroll
    for (int t = 0; t < 4; ++t) {
      PAD();
#pragma unroll
      for (int ks = 0; ks < 4; ++ks) {
        const v4u qm = qs == t ? qf[ks] : v4u{0u, 0u, 0u, 0u};
#pragma unroll
        for (int w = 0; w < 3; ++w) {
          sc[w] = mfma(kf[3 * ks + w], qm, sc[w]);
          if (t < 3) kf[3 * ks + w] = *lp<v4u>(sm, ko[ks] + w * 16384 + (t + 1) * 4096);
        }
        __builtin_amdgcn_sched_barrier(0);
      }
    }
    PAD();
    float mx = -__builtin_inff();
#pragma unroll
    for (int w = 0; w < 3; ++w) {
      const bool valid = 3 * j + w < nun;
#pragma unroll
      for (int r = 0; r < 4; r += 2) {
        const u32 s2 = pk2(sc[w][r], sc[w][r + 1]);
        u32 t2 = pk2(bflo(s2) * 0.08838834764831845f, bfhi(s2) * 0.08838834764831845f);
        PIN(t2);
        t2 = valid ? t2 : 0xff80ff80u;
        sc[w][r] = bflo(t2); sc[w][r + 1] = bfhi(t2);
        mx = fmaxf(mx, fmaxf(sc[w][r], sc[w][r + 1]));
      }
    }
    mx = fmaxf(mx, dppf<0xB1>(mx)); mx = fmaxf(mx, dppf<0x4E>(mx));
    mx = max32(max16(mx));
    *lp<float>(sm, L_MX + (u32)qb * 16u + j * 4) = mx;
    lds_add(sm, C_MX, lane, 1u);
    PAD();
    lds_wait<0>(sm, C_MX, 3);
    const v4f m0 = *lp<v4f>(sm, L_MX + (u32)qb * 16u);
    mx = fmaxf(fmaxf(m0[0], m0[1]), m0[2]);
    float ls = 0.f;
#pragma unroll
    for (int w = 0; w < 3; ++w) {
#pragma unroll
      for (int r = 0; r < 4; ++r) sc[w][r] = __expf(sc[w][r] - mx);
      ls += (sc[w][0] + sc[w][1]) + (sc[w][2] + sc[w][3]);
      *lp<v2u>(sm, L_P + (u32)(j * 1536 + w * 512 + (qs >> 1) * 256 + g * 64 + qb * 16 + (qs & 1) * 8)) = v2u{pk2(sc[w][0], sc[w][1]), pk2(sc[w][2], sc[w][3])};
    }
    PAD();
    ls = quadg_sum(ls);
    *lp<float>(sm, L_LS + (u32)qb * 16u + j * 4) = ls;
    const u32 order = lds_inc_ret(sm, C_LS, lane);
    if (FPM ? order < 2u && gi < 28 && !isWO : order == 0u && gi < 28) {
      const int fm = FPM ? min(sp, 3) * 2 + (int)order : sp;
      u32 t = get_word(sm, C_REF) + FT0 + (u32)((fm + bx) & 7) * FST, pf[3];
      PAD();
      sleep_until(t);
      lds_wait<2>(sm, C_ISS, nun);
#pragma unroll
      for (int i = 0; i < 3; ++i) {
        const int c = i * 8 + fm;
        const char* a = c < 16 ? (c < 8 ? pwg : pwu) + (u32)(((c & 7) * 64 + lane) * 3584 + gi * 128) : pwd + (u32)(gi * 65536 + (c & 7) * 8192 + lane * 128);
        sleep_until(t); t += i == 1 ? FS2 : FS;
        pf[i] = *(const GAS u32*)a;
        CBAR();
      }
#pragma unroll
      for (int i = 0; i < 3; ++i) asm volatile("" :: "v"(pf[i]));
    }
    if (order == 2u) {
      PAD();
      const v4f l0 = *lp<v4f>(sm, L_LS + (u32)qb * 16u);
      ls = (l0[0] + l0[1]) + l0[2];
      if ((lane & 0x33) == 0) put2(ws, WS_ML + (u32)((((bx * 4 + qb) * 4 + b) * 8 + sp) * 16), FU(mx), FU(ls), tag);
      if (isWO) {
        v4u ml[1];
        sleep_until(get_word(sm, C_REF) + MT1);
        gather2<1, 0, 8>(ws, WS_ML + (u32)(((hq * 4 + b) * 32 + (lane & 31)) * 16), tag, ml);
        PAD();
        const float ms = UF(ml[0].x);
        float M = fmaxf(ms, dppf<0xB1>(ms)); M = fmaxf(M, dppf<0x4E>(M)); M = fmaxf(M, dppf<0x141>(M));
        const float e = __expf(ms - M);
        float L = e * UF(ml[0].z);
        L += dppf<0xB1>(L); L += dppf<0x4E>(L); L += dppf<0x141>(L);
        *lp<float>(sm, L_WS + (u32)(lane & 31) * 4u) = e * __builtin_amdgcn_rcpf(L);
        lds_set(sm, C_W);
      }
    }
  } else {
    const bool main4 = wave >= 9;
    const u32 tag = get_tag(sm);
    if (wave == (isQ ? 7 : 12)) {
      sleep_until((u32)tstart + RT0);
      int d = (int)(gather1<8>(ws, WS_TG + (u32)(bx * 128 + (lane & 7) * 8), tag) - (u32)tstart);
      d = min(d, (int)dppu<0xB1>((u32)d)); d = min(d, (int)dppu<0x4E>((u32)d)); d = min(d, (int)dppu<0x141>((u32)d));
      *(volatile LAS v2u*)(sm + C_REF) = v2u{1u, (u32)tstart + (u32)max(min(d, 100), -200)};
    }
    if (wave == 12) {
      PAGE(); PADL(0);
      sleep_until(get_word(sm, C_REF) + QT0);
      v2u qg[2];
      v4u qsets[QR][2];
      const u32 qo = WS_QG + (u32)(b * 16384 + bx * 2048 + lane * 16);
      gatherp<2, 1024, QR, QGAP>(ws, qo, tag, qg, qsets);
      *lp<v2u>(sm, L_Q + (u32)(lane >> 5) * 320u + (u32)(lane & 31) * 8u) = qg[0];
      *lp<v2u>(sm, L_Q + 640 + (u32)(lane >> 5) * 320u + (u32)(lane & 31) * 8u) = qg[1];
      lds_set(sm, C_QRDY);
      keep(qsets);
    }
    if (isFFN ? main4 : isWO && wave >= 11) {
      const int head = hq * 4 + b, c8 = 2 * (sp - 3 - (sp >> 1 == 3)) + wave - 11;
      if (isFFN && wave < 11) {
        const u32 mo = mgr + (u32)(v * 24576 + 6144 + lane * 16);
        PAD();
        sleep_until((u32)tstart + SBT0);
        v4u mg[8];
        u32 left = SPIN_G, bad;
#pragma nounroll
        do {
          __builtin_amdgcn_s_sleep(8);
#pragma unroll
          for (int i = 0; i < 8; ++i) mg[i] = __builtin_amdgcn_raw_buffer_load_b128(ws, mo, (i >> 2) * 12288 + (i & 3) * 1024, 16);
          --left;
          bad = 0u;
#pragma unroll
          for (int i = 0; i < 8; ++i) bad |= tagbad(mg[i], tag);
          bad &= left ? ~0u : 0u;
        } while (anynz(bad));
        PAD();
#pragma unroll
        for (int i = 0; i < 8; ++i) {
          if (i & 2) *lp<v2u>(sm, L_PART + (u32)(v * 2048 + lane * 8 + (i >> 2) * 1024 + (i & 1) * 512)) =
                         v2u{pk2(1.f + bflo(mg[i].x), 1.f + bfhi(mg[i].x)), pk2(1.f + bflo(mg[i].z), 1.f + bfhi(mg[i].z))};
          else *lp<v2u>(sm, L_ACT + (u32)(v * 2176 + lane * 8 + (i >> 2) * 1088 + (i & 1) * 512)) = v2u{mg[i].x, mg[i].z};
        }
        lds_set(sm, F_S + (u32)v * 4u);
      }
      PAGE(); PAD();
      const u32 ref = get_word(sm, C_REF);
      sleep_until(ref + (isWO ? WA0 : GT0));
      lds_wait<2>(sm, isWO ? C_ISS : C_PUB, isWO ? nun : 4);
      v4u wf[16];
      {
        v4u wr[16];
        if (isWO) {
          const rsrc_t wo = mkrsrc(pwo + (u32)(head * 131072 + c8 * 128), 131072);
          u32 due = (ref + WA0) * 256u + (u32)(wave & 1) * (WAL * 8u);
#pragma unroll
          for (int j = 0; j < 16; ++j) {
            if ((j & 7) == 0) PAD();
            if (j % WPM == 0) pace256(due);
            due += WAL * 16u;
            wr[j] = __builtin_amdgcn_raw_buffer_load_b128(wo, (u32)((lane >> 3) * 8192 + (lane & 7) * 16), (j & 7) * 1024 + (j >> 3) * 65536, WNT * 2);
            __builtin_amdgcn_sched_barrier(0);
          }
        } else {
          const char* wp = ((lane & 4) ? pwu : pwg) + (u32)(((v >> 1) * 64 + (g & 1) * 32 + (n >> 3) * 16 + (v & 1) * 4 + (g >> 1)) * 3584 + fi * 64 + qs * 16);
          const u32 par = (u32)(bx & 1), flip = sp == 5;
          PAD();
#pragma unroll
          for (int j = 0; j < 16; ++j) {
            wr[j] = *(const GAS v4u*)(wp + (((u32)(j & 1) ^ par) * 8u + (u32)((j >> 1) & 3) * 128u + ((u32)(j >> 3) ^ flip) * 2u) * 3584u);
            __builtin_amdgcn_sched_barrier(0);
          }
        }
        CBAR();
        PAD();
        perm16(wr, wf);
      }
      PAD();
      u32 ao = L_ACT + (u32)(qb * 1088 + g * 32 + (qs & 1) * 16 + (isWO ? 0 : v * 256)), ao1 = ao + (sp == 5 ? 0u : 128u), fa = F_A + (u32)(lane & (isWO ? 7 : 3)) * 4u;
      u32 o = isWO ? WS_WP + (u32)(((g * 8 + c8) * 2 + (n >> 3)) * 2) * WPS + (u32)(head * 128 + (n & 7) * 16) : L_PART + (u32)(v * 1024 + g * 256 + qs * 64 + (n >> 3) * 32 + (qb & 1) * 8);
      u32 ff = F_F + (u32)v * 4u, one = 1u;
      v4u q0 = {0u, tag, 0u, tag}, q1 = q0;
      ao += sp == 5 ? 128u : 0u;
      PIN(ao); PIN(ao1); PIN(fa); PIN(o); PIN(ff); PIN(one); PIN(q0); PIN(q1);
      PAD();
      v4u a0, a1;
      {
        u32 left = SPIN_L, f, thr;
#pragma nounroll
        do {
          f = *(volatile LAS u32*)(sm + fa);
          a0 = *(volatile LAS v4u*)(sm + ao); a1 = *(volatile LAS v4u*)(sm + ao1);
          --left; thr = left ? 1u : 0u;
        } while (__builtin_amdgcn_ballot_w64(f < thr) != 0ull);
      }
      const v4f r = mma64(a0, a1, wf, n < 8);
      if (isWO) {
        q0.x = FU(r[0]); q0.z = FU(r[1]); q1.x = FU(r[2]); q1.z = FU(r[3]);
        __builtin_amdgcn_raw_buffer_store_b128(q0, ws, o, 0, 16);
        __builtin_amdgcn_raw_buffer_store_b128(q1, ws, o, WPS, 16);
      } else {
        *lp<v2f>(sm, o) = v2f{r[0], r[1]};
        *lp<v2f>(sm, o + 16) = v2f{r[2], r[3]};
        CBAR();
        *(volatile LAS u32*)(sm + ff) = one;
      }
    } else if (isQ ? wave == 9 : (sp == 6 && main4 && wave < 11)) {
      const int task = gi * 4 + (isQ ? sp - 1 : wave - 7), tb = task >> 5;
      const u32 pr = (u32)(((task >> 2) & 7) * 32 + (lane & 7) * 4 + (task & 3));
      const bool hi = lane >= 32;
      PAD();
      const u32 xp = *(const GAS u32*)(px + tb * 1024 + pr * 4);
      sleep_until((u32)tstart + SBT0);
      const u32 ga2 = gather1<8>(ws, mgr + (u32)(tb * 12288 + 4096) + pr * 8u, tag);
      const u32 gb2 = gather1<8>(ws, mgr + (u32)(tb * 12288 + 10240) + pr * 8u, tag);
      PAD();
      float xv = hi ? bfhi(xp) : bflo(xp), ga = hi ? bfhi(ga2) : bflo(ga2), gb = hi ? bfhi(gb2) : bflo(gb2);
      u32 oo = (u32)(tb * 1024) + pr * 4u + (u32)(lane >> 5) * 2u;
      const rsrc_t outr = mkrsrc(pout, 4096);
      PIN(gb); PIN(oo);
      u32 xo = WS_XG + (u32)((((lane >> 3) & 3) * 2 + (lane & 1)) * 16384 + task * 128 + (lane >> 5) * 64 + ((lane >> 1) & 3) * 16);
      v4u xq = {0u, tag, 0u, tag};
      PIN(xv); PIN(ga); PIN(xo); PIN(xq);
      const u32 ref = get_word(sm, C_REF) + OT0;
      float a0, a1;
      const u32 tk = gathersum<4>(ws, WS_WP + (u32)task * WPS + (u32)lane * 16u, tag, ref + PH_B, ref + PH_B, a0, a1) - ref - PH_B, tf = tk + ref + PH_F + RET_F;
      a0 = fold32(a0, a1);
      a0 += dppf<0x128>(a0);
      const float x1 = rb(xv + ga * sum16(a0));
      xq.x = FU(x1); xq.z = dppu<0xB1>(FU(x1));
      __builtin_amdgcn_raw_buffer_store_b128(xq, ws, xo, 0, 16);
      PAD();
      gathersum<7>(ws, WS_FP + (u32)task * FPS + (u32)lane * 16u, tag, tk ? tf : tf - RET_F, tf, a0, a1);
      a0 = fold32(a0, a1);
      a0 += dppf<0x128>(a0);
      const float y = x1 + gb * sum16(a0);
      __builtin_amdgcn_raw_buffer_store_b16((unsigned short)pk2(y, y), outr, oo, 0, 0);
      if (task == 0 && lane == 0) __builtin_amdgcn_raw_buffer_store_b32(tag, ws, WS_EPOCH, 0, 16);
    }
  }
  if (isWO && wave < 8) {
    __builtin_amdgcn_s_setprio(PRIO);
    const u32 tag = get_tag(sm);
    v4u og[2];
    PAD();
    float wgt[2];
    u32 wok = 0, left = SPIN_G, bad[2], any, one = 1u;
    u32 oo = WS_OP + (u32)(((hq * 4 + b) * 8 + wave) * 2048 + lane * 16), wo = L_WS + (u32)((wave >> 1) * 32 + g * 4);
    u32 lo = L_ACT + (u32)((wave >> 1) * 1088 + (wave & 1) * 128 + n * 8 + (lane >> 5) * 4 + (g & 1) * 2), fa = F_A + (u32)wave * 4u;
#pragma unroll
    for (int i = 0; i < 2; ++i) { og[i] = v4u{0u, 0u, 0u, 0u}; bad[i] = 1u; PIN(og[i]); PIN(bad[i]); }
    PIN(lo); PIN(fa); PIN(oo); PIN(wo); PIN(one);
    const u32 first = get_word(sm, C_REF) + OT0;
    sleep_until(first - OPRE);
#pragma nounroll
    do {
      slot_wait(first);
#pragma unroll
      for (int i = 0; i < 2; ++i)
        if (bad[i]) og[i] = __builtin_amdgcn_raw_buffer_load_b128(ws, oo, i * 1024, OAUX);
      CBAR();
      PAD();
      __builtin_amdgcn_s_sleep(WSL);
      wok = *(volatile LAS u32*)(sm + C_W);
#pragma unroll
      for (int i = 0; i < 2; ++i) wgt[i] = *(volatile LAS float*)(sm + wo + i * 16);
      --left;
      const u32 lim = left ? ~0u : 0u;
      wok = __builtin_amdgcn_readfirstlane(wok);
      asm volatile("" : "+s"(wok));
      __builtin_amdgcn_sched_barrier(0);
#pragma unroll
      for (int i = 0; i < 2; ++i) bad[i] = tagbad(og[i], tag);
      any = (bad[0] | bad[1]) & lim;
    } while (anynz(any));
    if (__builtin_expect(!wok, 0)) {
      lds_wait<0>(sm, C_W, 1);
#pragma unroll
      for (int i = 0; i < 2; ++i) wgt[i] = *(volatile LAS float*)(sm + wo + i * 16);
    }
    const v2f oe = v2f{bflo(og[0].x), bflo(og[0].z)} * wgt[0] + v2f{bflo(og[1].x), bflo(og[1].z)} * wgt[1];
    const v2f od = v2f{bfhi(og[0].x), bfhi(og[0].z)} * wgt[0] + v2f{bfhi(og[1].x), bfhi(og[1].z)} * wgt[1];
    const float of = fold16(fold32(oe[0], oe[1]), fold32(od[0], od[1]));
    *lp<unsigned short>(sm, lo) = (unsigned short)pk2(of, of);
    CBAR();
    *(volatile LAS u32*)(sm + fa) = one;
  }
}

extern "C" __device__ __attribute__((used, noinline, visibility("hidden"))) void fused_code_end() { PADEND(); }

extern "C" void kernel_launch(void* const* d_in, const int* in_sizes, int n_in,
                              void* d_out, int out_size, void* d_ws, size_t ws_size,
                              hipStream_t stream) {
  fused_decode_kernel<<<256, 1024, 0, stream>>>(
      (const char*)d_in[0], (const char*)d_in[1], (const char*)d_in[2], (const char*)d_in[3], (const char*)d_in[4],
      (const char*)d_in[6], (const char*)d_in[7], (const char*)d_in[8], (const char*)d_in[9], (const char*)d_in[10],
      (char*)d_out, (char*)d_ws);
}
```
